```python
import torch

def _softmax(x, axis=-1):
    return torch.softmax(x, dim=axis)
def _gelu(x):
    return torch.nn.functional.gelu(x, approximate="tanh")
def _silu(x):
    return torch.nn.functional.silu(x)
def _sigmoid(x):
    return torch.sigmoid(x)
def _layernorm(x, axis=-1, eps=1e-5):
    m = x.mean(dim=axis, keepdim=True)
    v = x.var(dim=axis, keepdim=True, unbiased=False)
    return (x - m) / torch.sqrt(v + eps)
def _scatter_add(tgt, idx, src, axis=0):
    return tgt.clone().index_add_(axis, idx, src)
def _segment_sum(src, seg, n):
    out = torch.zeros((n,) + src.shape[1:], dtype=src.dtype)
    return out.index_add_(0, seg, src)
def _pad(x, pw):
    flat = tuple(v for pair in reversed(pw) for v in pair)
    return torch.nn.functional.pad(x, flat)

kernel_name = 'parallel_sum__sliding_block__glu_gated_glu__prompt'

def setup_inputs():

    _w = (lambda a: (lambda t: t.to(torch.bfloat16) if t.is_floating_point() else t)(torch.as_tensor(a)))
    def _A_setup():
        B, S, D = 1, 2048, 512
        return {
            "x": _w((lambda *s: torch.randn(*s, dtype=torch.bfloat16) * 0.05)(B, S, D)),
            "wqkv": _w((lambda *s: torch.randn(*s, dtype=torch.bfloat16) * 0.05)(D, 3 * D)),
            "wo": _w((lambda *s: torch.randn(*s, dtype=torch.bfloat16) * 0.05)(D, D)),
        }
    _A_in = _A_setup()

    def _B_setup():
        B, S, D, F = 1, 2048, 512, 1792
        return {
            "x": _w((lambda *s: torch.randn(*s, dtype=torch.bfloat16) * 0.05)(B, S, D)),
            "w_gate": _w((lambda *s: torch.randn(*s, dtype=torch.bfloat16) * 0.05)(D, F)),
            "w_up": _w((lambda *s: torch.randn(*s, dtype=torch.bfloat16) * 0.05)(D, F)),
            "w_down": _w((lambda *s: torch.randn(*s, dtype=torch.bfloat16) * 0.05)(F, D)),
        }
    _B_in = _B_setup()

    return {
        "A_x": _A_in["x"],
        "A_wqkv": _A_in["wqkv"],
        "A_wo": _A_in["wo"],
        "B_x": _B_in["x"],
        "B_w_gate": _B_in["w_gate"],
        "B_w_up": _B_in["w_up"],
        "B_w_down": _B_in["w_down"],
    }

def reference(A_x, A_wqkv, A_wo, B_x, B_w_gate, B_w_up, B_w_down):
    def _A_ref(x, wqkv, wo):
        B, S, D = x.shape
        H = 32; Dh = D // H; W = 256
        qkv = (lambda x, *d: x.permute(*d))((x @ wqkv).reshape(B, S, 3, H, Dh), 2, 0, 3, 1, 4)
        q, k, v = qkv[0], qkv[1], qkv[2]
        scores = torch.einsum("bhqd,bhkd->bhqk", q, k) * (Dh ** -0.5)
        idx = torch.arange(S)
        mask = (idx[:, None] - idx[None, :] >= 0) & (idx[:, None] - idx[None, :] < W)
        scores = torch.where(mask[None, None], scores, -1e9)
        p = _softmax(scores, axis=-1)
        o = torch.einsum("bhqk,bhkd->bhqd", p, v)
        return (lambda x, *d: x.permute(*d))(o, 0, 2, 1, 3).reshape(B, S, D) @ wo

    def _B_ref(x, w_gate, w_up, w_down):
        g = torch.einsum("bsd,df->bsf", x, w_gate)
        u = torch.einsum("bsd,df->bsf", x, w_up)
        h = _sigmoid(g) * u
        return torch.einsum("bsf,fd->bsd", h, w_down)

    x = A_x
    xn = _layernorm(x, axis=-1)
    return x + _A_ref(xn, A_wqkv, A_wo) + _B_ref(xn, B_w_gate, B_w_up, B_w_down)
```

```cpp
#include <hip/hip_runtime.h>
#include <hip/hip_bf16.h>
typedef unsigned short u16;
__device__ __forceinline__ float bf2f(u16 b){ return __uint_as_float(((unsigned)b)<<16); }
__device__ __forceinline__ u16 f2bf(float f){ unsigned u=__float_as_uint(f); u += 0x7fffu + ((u>>16)&1u); return (u16)(u>>16); }
constexpr int S=2048, D=512, F=1792, H=32, DH=16, W=256;

__global__ void n_ln(const u16* x, float* xn){
  int r=blockIdx.x; __shared__ float red[64];
  float v[8]; float s=0; for(int i=0;i<8;i++){ v[i]=bf2f(x[r*D+threadIdx.x*8+i]); s+=v[i]; }
  for(int o=32;o;o>>=1) s+=__shfl_xor(s,o); float m=s/D; float q=0; for(int i=0;i<8;i++){ float d=v[i]-m; q+=d*d; }
  for(int o=32;o;o>>=1) q+=__shfl_xor(q,o); float rs=rsqrtf(q/D+1e-5f);
  for(int i=0;i<8;i++) xn[r*D+threadIdx.x*8+i]=(v[i]-m)*rs;
}
__global__ void n_gemm(const float* A, const u16* B, float* C, int M,int N,int K){
  int n=blockIdx.x*blockDim.x+threadIdx.x; int m0=blockIdx.y*8; if(n>=N) return;
  float acc[8]={0,0,0,0,0,0,0,0};
  for(int k=0;k<K;k++){ float b=bf2f(B[(size_t)k*N+n]);
    #pragma unroll
    for(int i=0;i<8;i++) acc[i]+=A[(size_t)(m0+i)*K+k]*b; }
  for(int i=0;i<8;i++) C[(size_t)(m0+i)*N+n]=acc[i];
}
__global__ void n_attn(const float* qkv, float* o){
  int q=blockIdx.x*blockDim.x+threadIdx.x; int h=blockIdx.y;
  float qv[DH]; for(int d=0;d<DH;d++) qv[d]=qkv[(size_t)q*3*D+h*DH+d];
  int k0=q-W+1; if(k0<0)k0=0; float m=-1e30f;
  for(int k=k0;k<=q;k++){ float s=0; for(int d=0;d<DH;d++) s+=qv[d]*qkv[(size_t)k*3*D+D+h*DH+d]; s*=0.25f; m=fmaxf(m,s);}
  float l=0; float acc[DH]; for(int d=0;d<DH;d++) acc[d]=0;
  for(int k=k0;k<=q;k++){ float s=0; for(int d=0;d<DH;d++) s+=qv[d]*qkv[(size_t)k*3*D+D+h*DH+d]; s*=0.25f; float p=__expf(s-m); l+=p; for(int d=0;d<DH;d++) acc[d]+=p*qkv[(size_t)k*3*D+2*D+h*DH+d]; }
  for(int d=0;d<DH;d++) o[(size_t)q*D+h*DH+d]=acc[d]/l;
}
__global__ void n_glu(const float* g,const float* u,float* h,int n){ int i=blockIdx.x*blockDim.x+threadIdx.x; if(i<n) h[i]=u[i]/(1.f+__expf(-g[i])); }
__global__ void n_sum(const u16* x,const float* a,const float* b,u16* out,int n){ int i=blockIdx.x*blockDim.x+threadIdx.x; if(i<n) out[i]=f2bf(bf2f(x[i])+a[i]+b[i]); }

#define NAIVE_NAME kernel_launch
extern "C" void NAIVE_NAME(void* const* d_in, const int* in_sizes, int n_in, void* d_out, int out_size, void* d_ws, size_t ws_size, hipStream_t stream){
  const u16* x=(const u16*)d_in[0]; const u16* wqkv=(const u16*)d_in[1]; const u16* wo=(const u16*)d_in[2];
  const u16* wg=(const u16*)d_in[4]; const u16* wu=(const u16*)d_in[5]; const u16* wd=(const u16*)d_in[6];
  float* ws=(float*)d_ws; float* xn=ws; float* qkv=xn+S*D; float* o=qkv+S*3*D; float* g=o+S*D; float* u=g+S*F; float* h=u+S*F; float* a=h+S*F; float* b=a+S*D;
  n_ln<<<S,64,0,stream>>>(x,xn);
  n_gemm<<<dim3(3*D/256,S/8),256,0,stream>>>(xn,wqkv,qkv,S,3*D,D);
  n_attn<<<dim3(S/64,H),64,0,stream>>>(qkv,o);
  n_gemm<<<dim3(D/256,S/8),256,0,stream>>>(o,wo,a,S,D,D);
  n_gemm<<<dim3(F/256,S/8),256,0,stream>>>(xn,wg,g,S,F,D);
  n_gemm<<<dim3(F/256,S/8),256,0,stream>>>(xn,wu,u,S,F,D);
  n_glu<<<S*F/256,256,0,stream>>>(g,u,h,S*F);
  n_gemm<<<dim3(D/256,S/8),256,0,stream>>>(h,wd,b,S,D,F);
  n_sum<<<S*D/256,256,0,stream>>>(x,a,b,(u16*)d_out,S*D);
}
```

```cpp
#include <hip/hip_runtime.h>

typedef unsigned int u32;
typedef unsigned long long u64;
typedef u32 u32x4 __attribute__((ext_vector_type(4)));
typedef u32 u32x2 __attribute__((ext_vector_type(2)));
typedef float f32x2 __attribute__((ext_vector_type(2)));
typedef float f32x4 __attribute__((ext_vector_type(4)));
typedef float f32x16 __attribute__((ext_vector_type(16)));
typedef __bf16 bf16x8 __attribute__((ext_vector_type(8)));
typedef __bf16 bf16x2 __attribute__((ext_vector_type(2)));
typedef short s16x4 __attribute__((ext_vector_type(4)));
#define LAS __attribute__((address_space(3)))
#define GAS __attribute__((address_space(1)))
#define DI __device__ __forceinline__
#define BC(T, v) __builtin_bit_cast(T, v)

constexpr u32 WS_Q = 0, WS_K = 2u << 20, WS_V = 4u << 20, WS_ACT = 6u << 20;
constexpr u32 WS_ST = 15u << 20;
constexpr u32 WS_CS = WS_ST + 32768;
constexpr u32 WS_F1 = WS_ST + 131072;
constexpr u32 WS_EP = WS_F1 + 1024;
constexpr u32 WS_F2 = WS_F1 + 2048;
constexpr u32 WS_FO = WS_F1 + 3072;
constexpr u32 WS_END = WS_F1 + 4096;
constexpr u32 WS_DBG = 16u << 20;
constexpr int NSTAMP = 12;

constexpr u32 SLOT = 16384;
constexpr u32 MISC = 9 * SLOT;
constexpr u32 L_R = MISC, L_MU = MISC + 512, L_RM = MISC + 1024, L_CS = MISC + 1536;
constexpr u32 L_CNT = MISC + 3072;
constexpr u32 L_CSP = MISC + 4096;
constexpr u32 L_ONE = MISC + 8192;
constexpr u32 L_KV = 6 * SLOT;
constexpr u32 LDS_BYTES = 163840;

constexpr float LOG2E = 1.4426950408889634f;
constexpr int SPIN = 1 << 20;

DI float asf(u32 v) { return __builtin_bit_cast(float, v); }
DI u32 asu(float v) { return __builtin_bit_cast(u32, v); }
DI f32x16 mfma(u32x4 a, u32x4 b, f32x16 c) {
  return __builtin_amdgcn_mfma_f32_32x32x16_bf16(BC(bf16x8, a), BC(bf16x8, b), c, 0, 0, 0);
}
DI u32x2 trr(u32 addr) { return BC(u32x2, __builtin_amdgcn_ds_read_tr16_b64_v4i16((LAS s16x4*)(size_t)addr)); }
DI u32x4 trr2(u32 a0, u32 a1) { u32x2 lo = trr(a0), hi = trr(a1); u32x4 r = {lo.x, lo.y, hi.x, hi.y}; return r; }
DI u32x4 lds128(u32 addr) { return *(LAS u32x4*)(size_t)addr; }
DI f32x4 lds128f(u32 addr) { return *(LAS f32x4*)(size_t)addr; }
DI float lds32f(u32 addr) { return *(LAS float*)(size_t)addr; }
DI void lds_st32f(u32 addr, float v) { *(LAS float*)(size_t)addr = v; }
DI void lds_st128(u32 addr, u32x4 v) { *(LAS u32x4*)(size_t)addr = v; }
DI u32 pk(float a, float b) { f32x2 v = {a, b}; return BC(u32, __builtin_convertvector(v, bf16x2)); }
DI float bflo(u32 v) { return BC(float, v << 16); }
DI float bfhi(u32 v) { return BC(float, v & 0xffff0000u); }
DI void glds(u32 voff, const char* sbase, u32 ldsaddr) {
  asm volatile("s_mov_b32 m0, %2\n\ts_nop 0\n\tglobal_load_lds_dwordx4 %0, %1" ::"v"(voff), "s"(sbase), "s"(ldsaddr) : "memory");
}
template <int N> DI void wait_vm() { asm volatile("s_waitcnt vmcnt(%0)" ::"i"(N) : "memory"); }
DI void bar() { asm volatile("s_waitcnt lgkmcnt(0)\n\ts_barrier" ::: "memory"); }
DI void sb() { __builtin_amdgcn_sched_barrier(0); }
DI u32x4 ld_sc1_asm(u32 voff, const char* sbase) {
  u32x4 r; asm volatile("global_load_dwordx4 %0, %1, %2 sc1" : "=v"(r) : "v"(voff), "s"(sbase) : "memory"); return r;
}
DI u32x4 ld_sc1_wait(u32 voff, const char* sbase) {
  u32x4 r; asm volatile("global_load_dwordx4 %0, %1, %2 sc1\n\ts_waitcnt vmcnt(0)" : "=&v"(r) : "v"(voff), "s"(sbase) : "memory"); return r;
}
DI float xsum32(float v) {
  u32x2 r = __builtin_amdgcn_permlane32_swap(asu(v), asu(v), false, false);
  return asf(r.x) + asf(r.y);
}
DI float xmax32(float v) {
  u32x2 r = __builtin_amdgcn_permlane32_swap(asu(v), asu(v), false, false);
  return fmaxf(asf(r.x), asf(r.y));
}
DI float wsum(float v) {
  v += BC(float, __builtin_amdgcn_update_dpp(0, BC(int, v), 0xB1, 0xf, 0xf, true));
  v += BC(float, __builtin_amdgcn_update_dpp(0, BC(int, v), 0x4E, 0xf, 0xf, true));
  v += BC(float, __builtin_amdgcn_update_dpp(0, BC(int, v), 0x141, 0xf, 0xf, true));
  v += BC(float, __builtin_amdgcn_update_dpp(0, BC(int, v), 0x140, 0xf, 0xf, true));
  u32x2 r = __builtin_amdgcn_permlane16_swap(asu(v), asu(v), false, false);
  v = asf(r.x) + asf(r.y);
  return xsum32(v);
}
DI float exp2c(float x) { return __builtin_amdgcn_fmed3f(__builtin_amdgcn_exp2f(x), 0.f, 1.f); }

constexpr u32 slot_of(int j) { return (u32)((j + 8) % 9) * SLOT; }

struct G1 {
  const char *xs, *a1, *b1, *w2s, *a3, *b3;
  const char *wd2, *wo2;
  u32 st1;
  u32 vx, v1, v2, v3, vw;
  u32 lw;
  u32 ax[4], aD0, aD1, aS;
  int w;
};

template <int J> DI void issue_block(const G1& g) {
  constexpr u32 so = slot_of(J);
  if constexpr (J < 16 && (J & 1) == 0) {
    const char* p = g.xs + (J / 2) * 128;
    glds(g.vx, p, so + g.lw); glds(g.vx, p + 65536, so + 8192 + g.lw);
  } else if constexpr (J < 16) {
    u32 o = g.st1 * (64 * (J / 2));
    glds(g.v1, g.a1 + o, so + g.lw); glds(g.v1 ^ 64u, g.b1 + o, so + 8192 + g.lw);
  } else if constexpr (J < 20) {
    const char* p = g.w2s + (J - 16) * 128 * 3072;
    glds(g.v2, p, so + g.lw); glds(g.v2, p + 64 * 3072, so + 8192 + g.lw);
  } else if constexpr (J < 28) {
    constexpr u32 o = 3584 * 64 * (J - 20);
    glds(g.v3, g.a3 + o, so + g.lw); glds(g.v3 ^ 64u, g.b3 + o, so + 8192 + g.lw);
  }
}
template <int I> DI void issue_w2(const G1& g) {
  constexpr u32 so = (u32)(I % 6) * SLOT;
  const char* p;
  if constexpr (I < 6) p = g.wd2 + (64 + 6 * g.w + I) * 16384;
  else if constexpr (I < 14) p = g.wd2 + (8 * g.w + (I - 6)) * 16384;
  else p = g.wo2 + (4 * g.w + (I - 14)) * 16384;
  glds(g.vw, p, so + 2 * g.lw); glds(g.vw, p + 8192, so + 2 * g.lw + 1024);
}

template <int S, int HF> DI void read_frags(const G1& g, u32x4 (&F)[4], u32x4 (&xf)[32]) {
  constexpr u32 so = slot_of(S < 8 ? 2 * S + 1 : 8 + S);
  if constexpr (S >= 8 && S < 12) {
    u32 a = g.aS + so;
    asm volatile("" : "+v"(a));
#pragma unroll
    for (int i = 0; i < 4; i++) F[i] = trr2(a + (16 * (4 * HF + i)) * 128, a + (16 * (4 * HF + i) + 4) * 128);
  } else {
    u32 a0 = g.aD0 + so, a1 = g.aD1 + so;
    asm volatile("" : "+v"(a0), "+v"(a1));
#pragma unroll
    for (int i = 0; i < 4; i++) {
      u32 a = (i & 1) ? a1 : a0;
      F[i] = trr2(a + (16 * (2 * HF + (i >> 1))) * 128, a + (16 * (2 * HF + (i >> 1)) + 4) * 128);
    }
  }
  if constexpr (S < 8) {
    constexpr u32 xo = slot_of(2 * S);
    u32 x0 = g.ax[2 * HF] + (xo & ~0xffffu), x1 = g.ax[2 * HF + 1] + (xo & ~0xffffu);
    if constexpr (xo >= 65536) asm volatile("" : "+v"(x0), "+v"(x1));
    xf[4 * S + 2 * HF] = lds128(x0 + (xo & 0xffffu));
    xf[4 * S + 2 * HF + 1] = lds128(x1 + (xo & 0xffffu));
  }
}
template <int S, int HF, bool VS> DI void do_mfma(u32x4 (&F)[4], u32x4 (&xf)[32], f32x16 (&acc)[2]) {
#pragma unroll
  for (int i = 0; i < 4; i++) {
    if constexpr (S >= 8 && S < 12) acc[0] = mfma(F[i], xf[8 * (S - 8) + 4 * HF + i], acc[0]);
    else {
      constexpr int su = S < 8 ? S : S - 12;
      u32x4 xv = xf[4 * su + 2 * HF + (i >> 1)];
      if constexpr (VS) acc[i & 1] = mfma(xv, F[i], acc[i & 1]);
      else acc[i & 1] = mfma(F[i], xv, acc[i & 1]);
    }
  }
}

#define STAMP(i)

__global__ __launch_bounds__(512, 2) void fused_block(const char* __restrict__ x, const char* __restrict__ wqkv,
                                                      const char* __restrict__ wo, const char* __restrict__ wg,
                                                      const char* __restrict__ wu, const char* __restrict__ wd,
                                                      char* __restrict__ out, char* __restrict__ ws) {
  __shared__ __attribute__((aligned(1024))) char smem[LDS_BYTES];
  STAMP(0);
  const u32 LB = (u32)(size_t)(LAS char*)smem;
  const int b = blockIdx.x, xcd = b & 7, idx = b >> 3;
  const int w = __builtin_amdgcn_readfirstlane(threadIdx.x >> 6);
  const u32 l = threadIdx.x & 63;
  const int m = w & 3, nh = w >> 2;
  const u32 r32 = l & 31, h = l >> 5;
  const int mi = (xcd & 1) * 8 + (idx & 7), nj = (xcd >> 1) * 4 + (idx >> 3);
  const bool isV = (nj & 3) == 3;
  const int p1 = 16 + (nj >> 2) * 3 + (nj & 3);
  const int vy = nj >> 2;
  const int m64 = xcd * 4 + (idx & 3), n64 = idx >> 2, mi2 = m64 >> 1;
  const int mia = idx & 15, hp = xcd * 2 + (idx >> 4);
  const __amdgpu_buffer_rsrc_t rs = __builtin_amdgcn_make_buffer_rsrc(ws, 0, (int)WS_END, 0x00020000);

  G1 g;
  g.w = w; g.lw = LB + w * 1024;
  g.xs = x + (size_t)mi * 131072;
  g.st1 = isV ? 3072 : 3584;
  g.a1 = isV ? wqkv + (16 + 2 * vy) * 128 : wg + p1 * 128;
  g.b1 = isV ? wqkv + (17 + 2 * vy) * 128 : wu + p1 * 128;
  g.w2s = wqkv + nj * 128;
  g.a3 = wg + nj * 128; g.b3 = wu + nj * 128;
  g.wd2 = wd + n64 * 128; g.wo2 = wo + n64 * 128;
  {
    u32 row = 8 * w + (l >> 3), p = l & 7, b4 = (l >> 4) & 1;
    g.vx = row * 1024 + ((p ^ (((w & 1) << 2) | ((l >> 4) & 3))) << 4);
    g.v1 = row * g.st1 + ((p ^ (b4 << 1)) << 4);
    g.v3 = row * 3584 + ((p ^ (b4 << 1)) << 4);
    g.v2 = row * 3072 + ((p ^ (b4 << 2)) << 4);
    g.vw = (l >> 3) * 1024 + ((p ^ (b4 << 2)) << 4);
  }
  u32 Ev;
  asm volatile("global_load_dword %0, %1, %2 sc1" : "=v"(Ev) : "v"(0u), "s"(ws + WS_EP) : "memory");
  const int srow = mi * 128 + nj * 8 + w;
  u32x4 xr;
  asm volatile("global_load_dwordx4 %0, %1, %2" : "=v"(xr) : "v"(l * 16), "s"(x + (size_t)srow * 1024) : "memory");
  const int cu = mi >> 1;
  const int gA1 = isV ? 16 + 2 * vy : 24 + p1, gB1 = isV ? 17 + 2 * vy : 52 + p1;
  auto group_of = [&](int u) { int t = u == 2 ? nj : nj + u * 28 - 60; t = u == 1 ? gB1 : t; return u == 0 ? gA1 : t; };
  const int cgrp = group_of(cu);
  const char* cb0 = cgrp < 52 ? wg - 24 * 128 : wu - 52 * 128;
  const char* cbase = (cgrp < 24 ? wqkv : cb0) + cgrp * 128;
  const u32 cst = cgrp < 24 ? 3072u : 3584u;
  u32x4 cw[4];
  if (mi < 10) {
    const char* cp = cbase + (size_t)((mi & 1) * 256 + w * 32) * cst;
    u32 vo = (l >> 3) * cst + (l & 7) * 16;
#pragma unroll
    for (int i = 0; i < 4; i++)
      asm volatile("global_load_dwordx4 %0, %1, %2" : "=v"(cw[i]) : "v"(vo), "s"(cp + (size_t)i * 8 * cst) : "memory");
  }
  issue_block<0>(g); issue_block<1>(g); issue_block<2>(g); issue_block<3>(g); issue_block<4>(g);
  issue_block<5>(g); issue_block<6>(g); issue_block<7>(g); issue_block<8>(g);
  {
    u32 gq = l >> 4, r = (l >> 2) & 3, c = l & 3, gb = gq & 1;
    u32 row = 32 * m + r32, f = (row >> 1) & 7;
#pragma unroll
    for (int q = 0; q < 4; q++) g.ax[q] = LB + row * 128 + (((2 * q + h) ^ f) << 4);
    u32 sw = ((r >> 1) & 1) | (gb << 1);
    g.aD0 = LB + gb * 8192 + (8 * h + r) * 128 + (((2 * nh) ^ sw) << 5) + c * 8;
    g.aD1 = g.aD0 ^ 32u;
    g.aS = LB + (8 * h + r) * 128 + ((nh ^ ((r >> 1) & 1)) << 6) + gb * 32 + c * 8;
  }
  if (threadIdx.x < 4) *(LAS u32*)(size_t)(LB + L_CNT + threadIdx.x * 4) = 0;
  asm volatile("s_waitcnt vmcnt(18)" : "+v"(Ev), "+v"(xr), "+v"(cw[0]), "+v"(cw[1]), "+v"(cw[2]), "+v"(cw[3])::"memory");
  const u32 T = __builtin_amdgcn_readfirstlane(Ev) + 0x9E3779B9u;
  {
    float v[8] = {bflo(xr.x), bfhi(xr.x), bflo(xr.y), bfhi(xr.y), bflo(xr.z), bfhi(xr.z), bflo(xr.w), bfhi(xr.w)};
    float s = ((v[0] + v[1]) + (v[2] + v[3])) + ((v[4] + v[5]) + (v[6] + v[7]));
    float mu = wsum(s) * (1.f / 512.f);
    float q = 0.f;
#pragma unroll
    for (int i = 0; i < 8; i++) { float d = v[i] - mu; q = fmaf(d, d, q); }
    float var = wsum(q) * (1.f / 512.f);
    float r = __builtin_amdgcn_rsqf(var + 1e-5f);
    if (l == 0) { u32x4 o = {asu(mu), T, asu(r), T}; __builtin_amdgcn_raw_buffer_store_b128(o, rs, WS_ST + srow * 16, 0, 16); }
  }
  if (mi < 10) {
    u32 one = (r32 & 1) ? 0x3f800000u : 0x00003f80u;
    u32x4 sel = {(r32 >> 1) == 0 ? one : 0u, (r32 >> 1) == 1 ? one : 0u, (r32 >> 1) == 2 ? one : 0u, (r32 >> 1) == 3 ? one : 0u};
    f32x16 c = {};
#pragma unroll
    for (int i = 0; i < 4; i++) c = mfma(cw[i], sel, c);
    if (r32 < 8) {
#pragma unroll
      for (int i = 0; i < 4; i++)
        lds_st32f(LB + L_CSP + w * 256 + ((i + 4 * h) * 8 + r32) * 4, (c[i] + c[i + 4]) + (c[i + 8] + c[i + 12]));
    }
  }
  u32x4 xf[32], F1[4], F2[4];
  f32x16 acc[2];
  wait_vm<14>(); bar();
  STAMP(1);
  if (mi < 10 && w == 7) {
    float s = 0.f;
#pragma unroll
    for (int i = 0; i < 8; i++) s += lds32f(LB + L_CSP + i * 256 + l * 4);
    const int grp = cgrp;
    u32x2 o = {asu(s), T};
    __builtin_amdgcn_raw_buffer_store_b64(o, rs, WS_CS + grp * 1024 + l * 16 + (mi & 1) * 8, 0, 16);
  }
  read_frags<0, 0>(g, F1, xf);

  u32 gsrc;
  {
    const int grp = group_of(w);
    gsrc = (w < 5 ? WS_CS + grp * 1024 : WS_ST + mi * 2048 + (w == 5 ? 0 : 1024)) + l * 16;
  }
  u32x4 gat, pol;

#define STAGE(S, VS, VMW, PRE, POST)                 \
  read_frags<S, 1>(g, F2, xf);                       \
  sb(); do_mfma<S, 0, VS>(F1, xf, acc); sb();        \
  if constexpr (VMW >= 0) wait_vm<(VMW >= 0 ? VMW : 0)>(); \
  PRE;                                               \
  bar();                                             \
  POST;                                              \
  if constexpr (S < 19) read_frags<(S < 19 ? S + 1 : 0), 0>(g, F1, xf); \
  sb(); do_mfma<S, 1, VS>(F2, xf, acc); sb();

  auto gather_consume = [&]() {
    asm volatile("" : "+v"(gat));
#pragma unroll 1
    for (int it = 0; it < SPIN; it++) {
      if (__all(gat.y == T && gat.w == T)) break;
      __builtin_amdgcn_s_sleep(2);
      gat = ld_sc1_wait(gsrc, ws);
    }
    float a = asf(gat.x), c = asf(gat.z);
    if (w < 5) {
      u32 i = w == 2 ? 128 + l : (w < 2 ? 0 : 192) + (l >> 4) * 32 + ((w == 1 || w == 4) ? 16 : 0) + (l & 15);
      lds_st32f(LB + L_CS + i * 4, a + c);
    } else if (w < 7) {
      u32 t = (w - 5) * 64 + l;
      lds_st32f(LB + L_MU + t * 4, a); lds_st32f(LB + L_R + t * 4, c); lds_st32f(LB + L_RM + t * 4, a * c);
    }
  };
#define UNIT1(VS)                                                                   \
  STAGE(0, VS, 10, , (issue_block<9>(g), issue_block<10>(g)))                        \
  STAGE(1, VS, 10, , (issue_block<11>(g), issue_block<12>(g)))                       \
  STAGE(2, VS, 10, , (issue_block<13>(g), issue_block<14>(g)))                       \
  STAGE(3, VS, 10, , (issue_block<15>(g), issue_block<16>(g), gat = ld_sc1_asm(gsrc, ws))) \
  STAGE(4, VS, 10, , (issue_block<17>(g), issue_block<18>(g)))                       \
  STAGE(5, VS, 10, , (issue_block<19>(g), issue_block<20>(g)))                       \
  STAGE(6, VS, 10, , (issue_block<21>(g), issue_block<22>(g)))                       \
  STAGE(7, VS, 12, gather_consume(), (issue_block<23>(g), issue_block<24>(g)))

  const int m32 = mi * 4 + m;
  float rr, mu;
  auto epi_gu = [&](u32 ub, int pair) {
    const float a = -LOG2E * rr, bb = LOG2E * rr * mu, rinv = __builtin_amdgcn_rcpf(rr);
#pragma unroll
    for (int f = 0; f < 2; f++) {
      const int fr = 2 * nh + f;
      f32x4 cs[4];
#pragma unroll
      for (int q = 0; q < 4; q++) cs[q] = lds128f(LB + L_CS + (ub + fr * 32 + q * 8 + 4 * h) * 4);
      float hv[8];
#pragma unroll
      for (int j = 0; j < 8; j++) {
        float e = __builtin_amdgcn_exp2f(fmaf(acc[f][j], a, bb * cs[j >> 2][j & 3]));
        float u = fmaf(-mu, cs[2 + (j >> 2)][j & 3], acc[f][j + 8]);
        hv[j] = u * __builtin_amdgcn_rcpf(fmaf(e, rinv, rinv));
      }
      u32x4 o = {pk(hv[0], hv[1]), pk(hv[2], hv[3]), pk(hv[4], hv[5]), pk(hv[6], hv[7])};
      __builtin_amdgcn_raw_buffer_store_b128(o, rs, (l * 16) + (WS_ACT + (m32 * 144 + 4 * pair + fr) * 1024), 0, 16);
    }
  };

  acc[0] = f32x16{}; acc[1] = f32x16{};
  if (isV) {
    UNIT1(true)
    rr = lds32f(LB + L_R + (32 * m + r32) * 4); mu = lds32f(LB + L_MU + (32 * m + r32) * 4);
#pragma unroll
    for (int f = 0; f < 2; f++) {
      const int fr = 2 * nh + f;
      float cs = lds32f(LB + L_CS + (fr * 32 + r32) * 4);
      u32 o[8];
#pragma unroll
      for (int q = 0; q < 4; q++) {
        f32x4 r4 = lds128f(LB + L_R + (32 * m + 8 * q + 4 * h) * 4), m4 = lds128f(LB + L_RM + (32 * m + 8 * q + 4 * h) * 4);
        float v[4];
#pragma unroll
        for (int i = 0; i < 4; i++) v[i] = fmaf(acc[f][4 * q + i], r4[i], -m4[i] * cs);
        o[2 * q] = pk(v[0], v[1]); o[2 * q + 1] = pk(v[2], v[3]);
      }
      const u32 head = (2 * vy + (r32 >> 4)) * 4 + fr;
      const u32 vo = head * 65536 + (h * 32 + (r32 & 15)) * 16;
      u32x4 o0 = {o[0], o[1], o[2], o[3]}, o1 = {o[4], o[5], o[6], o[7]};
      __builtin_amdgcn_raw_buffer_store_b128(o0, rs, (vo) + (WS_V + m32 * 1024), 0, 16);
      __builtin_amdgcn_raw_buffer_store_b128(o1, rs, (vo + 256) + (WS_V + m32 * 1024), 0, 16);
    }
  } else {
    UNIT1(false)
    rr = lds32f(LB + L_R + (32 * m + r32) * 4); mu = lds32f(LB + L_MU + (32 * m + r32) * 4);
    epi_gu(0, p1);
  }
  STAMP(2);
  acc[0] = f32x16{};
  STAGE(8, false, 14, , issue_block<25>(g))
  STAGE(9, false, 14, , issue_block<26>(g))
  STAGE(10, false, 14, , issue_block<27>(g))
  STAGE(11, false, 14, , issue_w2<0>(g))
  {
    const float sc = rr * (nj < 8 ? 0.25f * LOG2E : 1.f), bb = -sc * mu;
    f32x4 cs[4];
#pragma unroll
    for (int q = 0; q < 4; q++) cs[q] = lds128f(LB + L_CS + (128 + nh * 32 + q * 8 + 4 * h) * 4);
    u32 o[8];
#pragma unroll
    for (int j = 0; j < 8; j++)
      o[j] = pk(fmaf(acc[0][2 * j], sc, bb * cs[j >> 1][(2 * j) & 3]), fmaf(acc[0][2 * j + 1], sc, bb * cs[j >> 1][(2 * j + 1) & 3]));
    u32x4 o0 = {o[0], o[1], o[2], o[3]}, o1 = {o[4], o[5], o[6], o[7]};
    const u32 so = (u32)((nj * 4 + nh * 2) * 64 + m32) * 1024;
    __builtin_amdgcn_raw_buffer_store_b128(o0, rs, (l * 16) + (WS_Q + so), 0, 16);
    __builtin_amdgcn_raw_buffer_store_b128(o1, rs, (l * 16) + (WS_Q + so + 65536), 0, 16);
  }
  STAMP(3);
  acc[0] = f32x16{}; acc[1] = f32x16{};
  STAGE(12, false, 14, , issue_w2<1>(g))
  STAGE(13, false, 14, , )
  STAGE(14, false, 0, , ({ if (threadIdx.x == 0) __builtin_amdgcn_raw_buffer_store_b32(T, rs, WS_F1 + (mi * 16 + nj) * 4, 0, 16); issue_w2<2>(g); issue_w2<3>(g); }))
  STAGE(15, false, -1, , issue_w2<4>(g))
  STAGE(16, false, -1, , issue_w2<5>(g))
  STAGE(17, false, -1, , )
  STAGE(18, false, -1, , pol = ld_sc1_asm(l * 16, ws + WS_F1))
  STAGE(19, false, -1, , )
  STAMP(4);
  {
    const int ml = l >> 2;
    const bool need = b == 0 || (ml <= mia && ml + 2 >= mia) || ml == mi2;
    asm volatile("s_waitcnt vmcnt(0)" : "+v"(pol)::"memory");
#pragma unroll 1
    for (int it = 0; it < SPIN; it++) {
      if (__all(!need || (pol.x == T && pol.y == T && pol.z == T && pol.w == T))) break;
      __builtin_amdgcn_s_sleep(2);
      pol = ld_sc1_wait(l * 16, ws + WS_F1);
    }
    if (b == 0 && threadIdx.x == 0) __builtin_amdgcn_raw_buffer_store_b32(T, rs, WS_EP, 0, 16);
  }
  STAMP(5);
  const int qt = w & 3, hd = w >> 2, head = 2 * hp + hd, TQ = mia * 4 + qt;
  u32x4 kv[6], qf;
  {
    const int ty = w >> 2, sh = 2 * hp + ((w >> 1) & 1), t0 = mia * 4 - 8 + (w & 1) * 6;
#pragma unroll
    for (int i = 0; i < 6; i++) {
      int t = t0 + i; t = t < 0 ? 0 : t;
      kv[i] = __builtin_amdgcn_raw_buffer_load_b128(rs, l * 16, WS_K + ty * (2u << 20) + (sh * 64 + t) * 1024, 16);
    }
    qf = __builtin_amdgcn_raw_buffer_load_b128(rs, l * 16, WS_Q + (head * 64 + TQ) * 1024, 16);
  }
  epi_gu(192, nj);
  {
    const int ty = w >> 2, t0 = mia * 4 - 8 + (w & 1) * 6;
#pragma unroll
    for (int i = 0; i < 6; i++) {
      u32x4 v = kv[i];
      if (ty == 1 && t0 + i < 0) v = u32x4{0, 0, 0, 0};
      lds_st128(LB + L_KV + (w * 6 + i) * 1024 + l * 16, v);
    }
    if (threadIdx.x < 384) {
      const int t = threadIdx.x >> 5;
      const u32 one = (mia * 4 - 8 + t >= 0) ? 0x3f803f80u : 0u;
      lds_st128(LB + L_ONE + threadIdx.x * 16, u32x4{one, one, one, one});
    }
  }
  u32x4 wA[6][2], wB[6][2];
  u32 aW;
  {
    u32 gq = l >> 4, r = (l >> 2) & 3, c = l & 3;
    aW = LB + w * 2048 + (4 * h + r) * 128 + (((r >> 1) & 1) << 6) + (gq & 1) * 32 + c * 8;
  }
#pragma unroll
  for (int i = 0; i < 6; i++) {
    wA[i][0] = trr2(aW + i * SLOT, aW + i * SLOT + 1024);
    wA[i][1] = trr2((aW ^ 64u) + i * SLOT, (aW ^ 64u) + i * SLOT + 1024);
  }
  bar();
  issue_w2<6>(g); issue_w2<7>(g); issue_w2<8>(g); issue_w2<9>(g); issue_w2<10>(g); issue_w2<11>(g);
  STAMP(6);
  {
    u32x4 kf[9];
    const u32 ka = LB + L_KV + (hd * 12 + qt) * 1024 + l * 16;
#pragma unroll
    for (int i = 0; i < 9; i++) kf[i] = lds128(ka + i * 1024);
    const f32x16 zero = {};
    f32x16 sd = mfma(kf[8], qf, zero), sf = mfma(kf[0], qf, zero);
    if (TQ < 8) {
#pragma unroll
      for (int i = 0; i < 16; i++) sf[i] = -__builtin_inff();
    }
    f32x16 sm;
    float mx = -__builtin_inff();
#pragma unroll
    for (int i = 0; i < 16; i++) {
      const u32 kk = (i & 3) + 8 * (i >> 2) + 4 * h;
      sm[i] = kk <= r32 ? sd[i] : sf[i];
    }
#pragma unroll
    for (int i = 0; i < 16; i += 2) mx = fmaxf(fmaxf(mx, sm[i]), sm[i + 1]);
#pragma unroll
    for (int t = 1; t < 8; t++) {
      f32x16 s = mfma(kf[t], qf, zero);
      float mn = mx;
#pragma unroll
      for (int i = 0; i < 16; i += 2) mn = fmaxf(fmaxf(mn, s[i]), s[i + 1]);
      mx = (TQ - 8 + t >= 0) ? mn : mx;
    }
    mx = xmax32(mx);
    wait_vm<0>();
    if (l == 0) {
      u32 old = __hip_atomic_fetch_add((LAS u32*)(size_t)(LB + L_CNT), 1u, __ATOMIC_RELAXED, __HIP_MEMORY_SCOPE_WORKGROUP);
      if (old == 7) __builtin_amdgcn_raw_buffer_store_b32(T, rs, WS_F2 + (mi * 16 + nj) * 4, 0, 16);
    }
    STAMP(7);
    f32x16 nm;
#pragma unroll
    for (int i = 0; i < 16; i++) nm[i] = -mx;
    const bool vl = r32 < 16;
    const u32 vst = vl ? 1024u : 512u;
    u32 va = vl ? LB + L_KV + 24576 + (hd * 12 + qt) * 1024 + h * 512 + r32 * 16 : LB + L_ONE + qt * 512 + (r32 - 16) * 16;
    f32x16 oa = {};
    {
      u32 p[8], pd[8], pf[8];
#pragma unroll
      for (int j = 0; j < 8; j++) {
        p[j] = pk(exp2c(sm[2 * j] - mx), exp2c(sm[2 * j + 1] - mx));
        const u32 k0 = ((2 * j) & 3) + 8 * (j >> 1) + 4 * h;
        const u32 mk = (k0 <= r32 ? 0xffffu : 0u) | (k0 + 1 <= r32 ? 0xffff0000u : 0u);
        pd[j] = p[j] & mk; pf[j] = p[j] ^ pd[j];
      }
      oa = mfma(lds128(va), u32x4{pf[0], pf[1], pf[2], pf[3]}, oa);
      oa = mfma(lds128(va + 256), u32x4{pf[4], pf[5], pf[6], pf[7]}, oa);
      u32 v8 = va + 8 * vst;
      oa = mfma(lds128(v8), u32x4{pd[0], pd[1], pd[2], pd[3]}, oa);
      oa = mfma(lds128(v8 + 256), u32x4{pd[4], pd[5], pd[6], pd[7]}, oa);
    }
#pragma unroll
    for (int t = 1; t < 8; t++) {
      f32x16 s = mfma(kf[t], qf, nm);
      va += vst;
      u32 p[8];
#pragma unroll
      for (int j = 0; j < 8; j++) p[j] = pk(exp2c(s[2 * j]), exp2c(s[2 * j + 1]));
      oa = mfma(lds128(va), u32x4{p[0], p[1], p[2], p[3]}, oa);
      oa = mfma(lds128(va + 256), u32x4{p[4], p[5], p[6], p[7]}, oa);
    }
    const float il = __builtin_amdgcn_rcpf(oa[8]);
    u32x4 o = {pk(oa[0] * il, oa[1] * il), pk(oa[2] * il, oa[3] * il), pk(oa[4] * il, oa[5] * il), pk(oa[6] * il, oa[7] * il)};
    __builtin_amdgcn_raw_buffer_store_b128(o, rs, (l * 16) + (WS_ACT + (TQ * 144 + 112 + head) * 1024), 0, 16);
  }
#pragma unroll
  for (int i = 0; i < 6; i++) {
    wB[i][0] = trr2(aW + i * SLOT, aW + i * SLOT + 1024);
    wB[i][1] = trr2((aW ^ 64u) + i * SLOT, (aW ^ 64u) + i * SLOT + 1024);
  }
  asm volatile("s_waitcnt vmcnt(0) lgkmcnt(0)" ::: "memory");
  if (l == 0) {
    u32 old = __hip_atomic_fetch_add((LAS u32*)(size_t)(LB + L_CNT + 4), 1u, __ATOMIC_RELAXED, __HIP_MEMORY_SCOPE_WORKGROUP);
    if (old == 7) __builtin_amdgcn_raw_buffer_store_b32(T, rs, WS_FO + (mia * 16 + hp) * 4, 0, 16);
  }
  STAMP(8);
  f32x16 c2[2][2] = {};
  const u32 abase = WS_ACT + (u32)(m64 * 2) * 144 * 1024;
  auto lda = [&](int mt, int ks) { return __builtin_amdgcn_raw_buffer_load_b128(rs, l * 16, abase + (mt * 144 + ks) * 1024, 16); };
  auto poll2 = [&](u32 off) {
#pragma unroll 1
    for (int it = 0; it < SPIN; it++) {
      u64 v = __hip_atomic_load((GAS u64*)(ws + off), __ATOMIC_RELAXED, __HIP_MEMORY_SCOPE_AGENT);
      if (__builtin_amdgcn_readfirstlane((u32)v) == T && __builtin_amdgcn_readfirstlane((u32)(v >> 32)) == T) break;
      __builtin_amdgcn_s_sleep(2);
    }
    asm volatile("" ::: "memory");
  };
  poll2(WS_F2 + (mi2 * 16 + 2 * w) * 4);
  issue_w2<12>(g); issue_w2<13>(g); issue_w2<14>(g); issue_w2<15>(g); issue_w2<16>(g); issue_w2<17>(g);
  {
    u32x4 a[6][2];
#pragma unroll
    for (int i = 0; i < 6; i++) { a[i][0] = lda(0, 64 + 6 * w + i); a[i][1] = lda(1, 64 + 6 * w + i); }
    u32x4 a2[8][2];
#pragma unroll
    for (int i = 0; i < 6; i++) {
      a2[i][0] = lda(0, 8 * w + i); a2[i][1] = lda(1, 8 * w + i);
#pragma unroll
      for (int mt = 0; mt < 2; mt++)
#pragma unroll
        for (int nt = 0; nt < 2; nt++) c2[mt][nt] = mfma(a[i][mt], wA[i][nt], c2[mt][nt]);
    }
    a2[6][0] = lda(0, 8 * w + 6); a2[6][1] = lda(1, 8 * w + 6);
    a2[7][0] = lda(0, 8 * w + 7); a2[7][1] = lda(1, 8 * w + 7);
#pragma unroll
    for (int i = 0; i < 6; i++)
#pragma unroll
      for (int mt = 0; mt < 2; mt++)
#pragma unroll
        for (int nt = 0; nt < 2; nt++) c2[mt][nt] = mfma(a2[i][mt], wB[i][nt], c2[mt][nt]);
    wait_vm<4>();
#pragma unroll
    for (int i = 0; i < 6; i++) {
      wA[i][0] = trr2(aW + i * SLOT, aW + i * SLOT + 1024);
      wA[i][1] = trr2((aW ^ 64u) + i * SLOT, (aW ^ 64u) + i * SLOT + 1024);
    }
#pragma unroll
    for (int i = 0; i < 2; i++)
#pragma unroll
      for (int mt = 0; mt < 2; mt++)
#pragma unroll
        for (int nt = 0; nt < 2; nt++) c2[mt][nt] = mfma(a2[6 + i][mt], wA[i][nt], c2[mt][nt]);
  }
  STAMP(9);
  poll2(WS_FO + (mi2 * 16 + 2 * w) * 4);
  STAMP(10);
  const u32 uu = threadIdx.x >> 3, oc = threadIdx.x & 7;
  const u32 tok = (uu & 1) * 4 + ((uu >> 1) & 3) + (uu >> 3) * 8;
  const size_t ooff = (size_t)(m64 * 64 + tok) * 1024 + n64 * 128 + oc * 16;
  u32x4 xres = *(const GAS u32x4*)(x + ooff);
  {
    u32x4 a[4][2];
#pragma unroll
    for (int i = 0; i < 4; i++) { a[i][0] = lda(0, 112 + 4 * w + i); a[i][1] = lda(1, 112 + 4 * w + i); }
#pragma unroll
    for (int i = 0; i < 4; i++)
#pragma unroll
      for (int mt = 0; mt < 2; mt++)
#pragma unroll
        for (int nt = 0; nt < 2; nt++) c2[mt][nt] = mfma(a[i][mt], wA[2 + i][nt], c2[mt][nt]);
  }
  bar();
#pragma unroll
  for (int mt = 0; mt < 2; mt++)
#pragma unroll
    for (int nt = 0; nt < 2; nt++)
#pragma unroll
      for (int i = 0; i < 16; i++) lds_st32f(LB + w * 16384 + (mt * 2 + nt) * 4096 + i * 256 + l * 4, c2[mt][nt][i]);
  bar();
  {
    const u32 t5 = tok & 31, reg = (t5 & 3) + 4 * (t5 >> 3), hh = (t5 >> 2) & 1;
    const u32 sw = (uu >> 1) & 1;
    const u32 ra = LB + ((tok >> 5) * 2 + (oc >> 2)) * 4096 + reg * 256 + hh * 128 + (oc & 3) * 32;
    f32x4 s0 = {}, s1 = {};
#pragma unroll
    for (int i = 0; i < 8; i++) { s0 += lds128f(ra + i * 16384 + sw * 16); s1 += lds128f(ra + i * 16384 + (sw ^ 1) * 16); }
    f32x4 lo = sw ? s1 : s0, hi = sw ? s0 : s1;
    u32x4 o = {pk(lo[0] + bflo(xres.x), lo[1] + bfhi(xres.x)), pk(lo[2] + bflo(xres.y), lo[3] + bfhi(xres.y)),
               pk(hi[0] + bflo(xres.z), hi[1] + bfhi(xres.z)), pk(hi[2] + bflo(xres.w), hi[3] + bfhi(xres.w))};
    *(GAS u32x4*)(out + ooff) = o;
  }
}

extern "C" void kernel_launch(void* const* d_in, const int* in_sizes, int n_in, void* d_out, int out_size, void* d_ws,
                              size_t ws_size, hipStream_t stream) {
  fused_block<<<256, 512, 0, stream>>>((const char*)d_in[0], (const char*)d_in[1], (const char*)d_in[2], (const char*)d_in[4],
                                       (const char*)d_in[5], (const char*)d_in[6], (char*)d_out, (char*)d_ws);
}
```

```cpp
#define STAMPS
#define PROBE_A -1
#define PROBE_B 0
#include <hip/hip_runtime.h>

typedef unsigned int u32;
typedef unsigned long long u64;
typedef u32 u32x4 __attribute__((ext_vector_type(4)));
typedef u32 u32x2 __attribute__((ext_vector_type(2)));
typedef float f32x2 __attribute__((ext_vector_type(2)));
typedef float f32x4 __attribute__((ext_vector_type(4)));
typedef float f32x16 __attribute__((ext_vector_type(16)));
typedef __bf16 bf16x8 __attribute__((ext_vector_type(8)));
typedef __bf16 bf16x2 __attribute__((ext_vector_type(2)));
typedef short s16x4 __attribute__((ext_vector_type(4)));
#define LAS __attribute__((address_space(3)))
#define GAS __attribute__((address_space(1)))
#define DI __device__ __forceinline__
#define BC(T, v) __builtin_bit_cast(T, v)

constexpr u32 WS_Q = 0, WS_K = 2u << 20, WS_V = 4u << 20, WS_ACT = 6u << 20;
constexpr u32 WS_ST = 15u << 20;
constexpr u32 WS_CS = WS_ST + 32768;
constexpr u32 WS_F1 = WS_ST + 131072;
constexpr u32 WS_EP = WS_F1 + 1024;
constexpr u32 WS_F2 = WS_F1 + 2048;
constexpr u32 WS_FO = WS_F1 + 3072;
constexpr u32 WS_END = WS_F1 + 4096;
constexpr u32 WS_DBG = 16u << 20;
constexpr int NSTAMP = 12;

constexpr u32 SLOT = 16384;
constexpr u32 MISC = 9 * SLOT;
constexpr u32 L_R = MISC, L_MU = MISC + 512, L_RM = MISC + 1024, L_CS = MISC + 1536;
constexpr u32 L_CNT = MISC + 3072;
constexpr u32 L_CSP = MISC + 4096;
constexpr u32 L_ONE = MISC + 8192;
constexpr u32 L_KV = 6 * SLOT;
constexpr u32 LDS_BYTES = 163840;

constexpr float LOG2E = 1.4426950408889634f;
constexpr int SPIN = 1 << 20;

DI float asf(u32 v) { return __builtin_bit_cast(float, v); }
DI u32 asu(float v) { return __builtin_bit_cast(u32, v); }
DI f32x16 mfma(u32x4 a, u32x4 b, f32x16 c) {
  return __builtin_amdgcn_mfma_f32_32x32x16_bf16(BC(bf16x8, a), BC(bf16x8, b), c, 0, 0, 0);
}
DI u32x2 trr(u32 addr) { return BC(u32x2, __builtin_amdgcn_ds_read_tr16_b64_v4i16((LAS s16x4*)(size_t)addr)); }
DI u32x4 trr2(u32 a0, u32 a1) { u32x2 lo = trr(a0), hi = trr(a1); u32x4 r = {lo.x, lo.y, hi.x, hi.y}; return r; }
DI u32x4 lds128(u32 addr) { return *(LAS u32x4*)(size_t)addr; }
DI f32x4 lds128f(u32 addr) { return *(LAS f32x4*)(size_t)addr; }
DI float lds32f(u32 addr) { return *(LAS float*)(size_t)addr; }
DI void lds_st32f(u32 addr, float v) { *(LAS float*)(size_t)addr = v; }
DI void lds_st128(u32 addr, u32x4 v) { *(LAS u32x4*)(size_t)addr = v; }
DI u32 pk(float a, float b) { f32x2 v = {a, b}; return BC(u32, __builtin_convertvector(v, bf16x2)); }
DI float bflo(u32 v) { return BC(float, v << 16); }
DI float bfhi(u32 v) { return BC(float, v & 0xffff0000u); }
DI void glds(u32 voff, const char* sbase, u32 ldsaddr) {
  asm volatile("s_mov_b32 m0, %2\n\ts_nop 0\n\tglobal_load_lds_dwordx4 %0, %1" ::"v"(voff), "s"(sbase), "s"(ldsaddr) : "memory");
}
template <int N> DI void wait_vm() { asm volatile("s_waitcnt vmcnt(%0)" ::"i"(N) : "memory"); }
DI void bar() { asm volatile("s_waitcnt lgkmcnt(0)\n\ts_barrier" ::: "memory"); }
DI void sb() { __builtin_amdgcn_sched_barrier(0); }
DI u32x4 ld_sc1_asm(u32 voff, const char* sbase) {
  u32x4 r; asm volatile("global_load_dwordx4 %0, %1, %2 sc1" : "=v"(r) : "v"(voff), "s"(sbase) : "memory"); return r;
}
DI u32x4 ld_sc1_wait(u32 voff, const char* sbase) {
  u32x4 r; asm volatile("global_load_dwordx4 %0, %1, %2 sc1\n\ts_waitcnt vmcnt(0)" : "=&v"(r) : "v"(voff), "s"(sbase) : "memory"); return r;
}
DI float xsum32(float v) {
  u32x2 r = __builtin_amdgcn_permlane32_swap(asu(v), asu(v), false, false);
  return asf(r.x) + asf(r.y);
}
DI float xmax32(float v) {
  u32x2 r = __builtin_amdgcn_permlane32_swap(asu(v), asu(v), false, false);
  return fmaxf(asf(r.x), asf(r.y));
}
DI float wsum(float v) {
  v += BC(float, __builtin_amdgcn_update_dpp(0, BC(int, v), 0xB1, 0xf, 0xf, true));
  v += BC(float, __builtin_amdgcn_update_dpp(0, BC(int, v), 0x4E, 0xf, 0xf, true));
  v += BC(float, __builtin_amdgcn_update_dpp(0, BC(int, v), 0x141, 0xf, 0xf, true));
  v += BC(float, __builtin_amdgcn_update_dpp(0, BC(int, v), 0x140, 0xf, 0xf, true));
  u32x2 r = __builtin_amdgcn_permlane16_swap(asu(v), asu(v), false, false);
  v = asf(r.x) + asf(r.y);
  return xsum32(v);
}
DI float exp2c(float x) { return __builtin_amdgcn_fmed3f(__builtin_amdgcn_exp2f(x), 0.f, 1.f); }

constexpr u32 slot_of(int j) { return (u32)((j + 8) % 9) * SLOT; }

struct G1 {
  const char *xs, *a1, *b1, *w2s, *a3, *b3;
  const char *wd2, *wo2;
  u32 st1;
  u32 vx, v1, v2, v3, vw;
  u32 lw;
  u32 ax[4], aD0, aD1, aS;
  int w;
};

template <int J> DI void issue_block(const G1& g) {
  constexpr u32 so = slot_of(J);
  if constexpr (J < 16 && (J & 1) == 0) {
    const char* p = g.xs + (J / 2) * 128;
    glds(g.vx, p, so + g.lw); glds(g.vx, p + 65536, so + 8192 + g.lw);
  } else if constexpr (J < 16) {
    u32 o = g.st1 * (64 * (J / 2));
    glds(g.v1, g.a1 + o, so + g.lw); glds(g.v1 ^ 64u, g.b1 + o, so + 8192 + g.lw);
  } else if constexpr (J < 20) {
    const char* p = g.w2s + (J - 16) * 128 * 3072;
    glds(g.v2, p, so + g.lw); glds(g.v2, p + 64 * 3072, so + 8192 + g.lw);
  } else if constexpr (J < 28) {
    constexpr u32 o = 3584 * 64 * (J - 20);
    glds(g.v3, g.a3 + o, so + g.lw); glds(g.v3 ^ 64u, g.b3 + o, so + 8192 + g.lw);
  }
}
template <int I> DI void issue_w2(const G1& g) {
  constexpr u32 so = (u32)(I % 6) * SLOT;
  const char* p;
  if constexpr (I < 6) p = g.wd2 + (64 + 6 * g.w + I) * 16384;
  else if constexpr (I < 14) p = g.wd2 + (8 * g.w + (I - 6)) * 16384;
  else p = g.wo2 + (4 * g.w + (I - 14)) * 16384;
  glds(g.vw, p, so + 2 * g.lw); glds(g.vw, p + 8192, so + 2 * g.lw + 1024);
}

template <int S, int HF> DI void read_frags(const G1& g, u32x4 (&F)[4], u32x4 (&xf)[32]) {
  constexpr u32 so = slot_of(S < 8 ? 2 * S + 1 : 8 + S);
  if constexpr (S >= 8 && S < 12) {
    u32 a = g.aS + so;
    asm volatile("" : "+v"(a));
#pragma unroll
    for (int i = 0; i < 4; i++) F[i] = trr2(a + (16 * (4 * HF + i)) * 128, a + (16 * (4 * HF + i) + 4) * 128);
  } else {
    u32 a0 = g.aD0 + so, a1 = g.aD1 + so;
    asm volatile("" : "+v"(a0), "+v"(a1));
#pragma unroll
    for (int i = 0; i < 4; i++) {
      u32 a = (i & 1) ? a1 : a0;
      F[i] = trr2(a + (16 * (2 * HF + (i >> 1))) * 128, a + (16 * (2 * HF + (i >> 1)) + 4) * 128);
    }
  }
  if constexpr (S < 8) {
    constexpr u32 xo = slot_of(2 * S);
    u32 x0 = g.ax[2 * HF] + (xo & ~0xffffu), x1 = g.ax[2 * HF + 1] + (xo & ~0xffffu);
    if constexpr (xo >= 65536) asm volatile("" : "+v"(x0), "+v"(x1));
    xf[4 * S + 2 * HF] = lds128(x0 + (xo & 0xffffu));
    xf[4 * S + 2 * HF + 1] = lds128(x1 + (xo & 0xffffu));
  }
}
template <int S, int HF, bool VS> DI void do_mfma(u32x4 (&F)[4], u32x4 (&xf)[32], f32x16 (&acc)[2]) {
#pragma unroll
  for (int i = 0; i < 4; i++) {
    if constexpr (S >= 8 && S < 12) acc[0] = mfma(F[i], xf[8 * (S - 8) + 4 * HF + i], acc[0]);
    else {
      constexpr int su = S < 8 ? S : S - 12;
      u32x4 xv = xf[4 * su + 2 * HF + (i >> 1)];
      if constexpr (VS) acc[i & 1] = mfma(xv, F[i], acc[i & 1]);
      else acc[i & 1] = mfma(F[i], xv, acc[i & 1]);
    }
  }
}

#define STAMP(i) stamp[i] = __builtin_amdgcn_s_memrealtime()

__global__ __launch_bounds__(512, 2) void fused_block(const char* __restrict__ x, const char* __restrict__ wqkv,
                                                      const char* __restrict__ wo, const char* __restrict__ wg,
                                                      const char* __restrict__ wu, const char* __restrict__ wd,
                                                      char* __restrict__ out, char* __restrict__ ws) {
  __shared__ __attribute__((aligned(1024))) char smem[LDS_BYTES];
  u64 stamp[NSTAMP];
  STAMP(0);
  u64 cyc0 = __builtin_amdgcn_s_memtime();
  const u32 LB = (u32)(size_t)(LAS char*)smem;
  const int b = blockIdx.x, xcd = b & 7, idx = b >> 3;
  const int w = __builtin_amdgcn_readfirstlane(threadIdx.x >> 6);
  const u32 l = threadIdx.x & 63;
  const int m = w & 3, nh = w >> 2;
  const u32 r32 = l & 31, h = l >> 5;
  const int mi = (xcd & 1) * 8 + (idx & 7), nj = (xcd >> 1) * 4 + (idx >> 3);
  const bool isV = (nj & 3) == 3;
  const int p1 = 16 + (nj >> 2) * 3 + (nj & 3);
  const int vy = nj >> 2;
  const int m64 = xcd * 4 + (idx & 3), n64 = idx >> 2, mi2 = m64 >> 1;
  const int mia = idx & 15, hp = xcd * 2 + (idx >> 4);
  const __amdgpu_buffer_rsrc_t rs = __builtin_amdgcn_make_buffer_rsrc(ws, 0, (int)WS_END, 0x00020000);

  G1 g;
  g.w = w; g.lw = LB + w * 1024;
  g.xs = x + (size_t)mi * 131072;
  g.st1 = isV ? 3072 : 3584;
  g.a1 = isV ? wqkv + (16 + 2 * vy) * 128 : wg + p1 * 128;
  g.b1 = isV ? wqkv + (17 + 2 * vy) * 128 : wu + p1 * 128;
  g.w2s = wqkv + nj * 128;
  g.a3 = wg + nj * 128; g.b3 = wu + nj * 128;
  g.wd2 = wd + n64 * 128; g.wo2 = wo + n64 * 128;
  {
    u32 row = 8 * w + (l >> 3), p = l & 7, b4 = (l >> 4) & 1;
    g.vx = row * 1024 + ((p ^ (((w & 1) << 2) | ((l >> 4) & 3))) << 4);
    g.v1 = row * g.st1 + ((p ^ (b4 << 1)) << 4);
    g.v3 = row * 3584 + ((p ^ (b4 << 1)) << 4);
    g.v2 = row * 3072 + ((p ^ (b4 << 2)) << 4);
    g.vw = (l >> 3) * 1024 + ((p ^ (b4 << 2)) << 4);
  }
  u32 Ev;
  asm volatile("global_load_dword %0, %1, %2 sc1" : "=v"(Ev) : "v"(0u), "s"(ws + WS_EP) : "memory");
  const int srow = mi * 128 + nj * 8 + w;
  u32x4 xr;
  asm volatile("global_load_dwordx4 %0, %1, %2" : "=v"(xr) : "v"(l * 16), "s"(x + (size_t)srow * 1024) : "memory");
  const int cu = mi >> 1;
  const int gA1 = isV ? 16 + 2 * vy : 24 + p1, gB1 = isV ? 17 + 2 * vy : 52 + p1;
  auto group_of = [&](int u) { int t = u == 2 ? nj : nj + u * 28 - 60; t = u == 1 ? gB1 : t; return u == 0 ? gA1 : t; };
  const int cgrp = group_of(cu);
  const char* cb0 = cgrp < 52 ? wg - 24 * 128 : wu - 52 * 128;
  const char* cbase = (cgrp < 24 ? wqkv : cb0) + cgrp * 128;
  const u32 cst = cgrp < 24 ? 3072u : 3584u;
  u32x4 cw[4];
  if (mi < 10) {
    const char* cp = cbase + (size_t)((mi & 1) * 256 + w * 32) * cst;
    u32 vo = (l >> 3) * cst + (l & 7) * 16;
#pragma unroll
    for (int i = 0; i < 4; i++)
      asm volatile("global_load_dwordx4 %0, %1, %2" : "=v"(cw[i]) : "v"(vo), "s"(cp + (size_t)i * 8 * cst) : "memory");
  }
  issue_block<0>(g); issue_block<1>(g); issue_block<2>(g); issue_block<3>(g); issue_block<4>(g);
  issue_block<5>(g); issue_block<6>(g); issue_block<7>(g); issue_block<8>(g);
  {
    u32 gq = l >> 4, r = (l >> 2) & 3, c = l & 3, gb = gq & 1;
    u32 row = 32 * m + r32, f = (row >> 1) & 7;
#pragma unroll
    for (int q = 0; q < 4; q++) g.ax[q] = LB + row * 128 + (((2 * q + h) ^ f) << 4);
    u32 sw = ((r >> 1) & 1) | (gb << 1);
    g.aD0 = LB + gb * 8192 + (8 * h + r) * 128 + (((2 * nh) ^ sw) << 5) + c * 8;
    g.aD1 = g.aD0 ^ 32u;
    g.aS = LB + (8 * h + r) * 128 + ((nh ^ ((r >> 1) & 1)) << 6) + gb * 32 + c * 8;
  }
  if (threadIdx.x < 4) *(LAS u32*)(size_t)(LB + L_CNT + threadIdx.x * 4) = 0;
  asm volatile("s_waitcnt vmcnt(18)" : "+v"(Ev), "+v"(xr), "+v"(cw[0]), "+v"(cw[1]), "+v"(cw[2]), "+v"(cw[3])::"memory");
  const u32 T = __builtin_amdgcn_readfirstlane(Ev) + 0x9E3779B9u;
  {
    float v[8] = {bflo(xr.x), bfhi(xr.x), bflo(xr.y), bfhi(xr.y), bflo(xr.z), bfhi(xr.z), bflo(xr.w), bfhi(xr.w)};
    float s = ((v[0] + v[1]) + (v[2] + v[3])) + ((v[4] + v[5]) + (v[6] + v[7]));
    float mu = wsum(s) * (1.f / 512.f);
    float q = 0.f;
#pragma unroll
    for (int i = 0; i < 8; i++) { float d = v[i] - mu; q = fmaf(d, d, q); }
    float var = wsum(q) * (1.f / 512.f);
    float r = __builtin_amdgcn_rsqf(var + 1e-5f);
    if (l == 0) { u32x4 o = {asu(mu), T, asu(r), T}; __builtin_amdgcn_raw_buffer_store_b128(o, rs, WS_ST + srow * 16, 0, 16); }
  }
  if (mi < 10) {
    u32 one = (r32 & 1) ? 0x3f800000u : 0x00003f80u;
    u32x4 sel = {(r32 >> 1) == 0 ? one : 0u, (r32 >> 1) == 1 ? one : 0u, (r32 >> 1) == 2 ? one : 0u, (r32 >> 1) == 3 ? one : 0u};
    f32x16 c = {};
#pragma unroll
    for (int i = 0; i < 4; i++) c = mfma(cw[i], sel, c);
    if (r32 < 8) {
#pragma unroll
      for (int i = 0; i < 4; i++)
        lds_st32f(LB + L_CSP + w * 256 + ((i + 4 * h) * 8 + r32) * 4, (c[i] + c[i + 4]) + (c[i + 8] + c[i + 12]));
    }
  }
  u32x4 xf[32], F1[4], F2[4];
  f32x16 acc[2];
  wait_vm<14>(); bar();
  STAMP(1);
  if (mi < 10 && w == 7) {
    float s = 0.f;
#pragma unroll
    for (int i = 0; i < 8; i++) s += lds32f(LB + L_CSP + i * 256 + l * 4);
    const int grp = cgrp;
    u32x2 o = {asu(s), T};
    __builtin_amdgcn_raw_buffer_store_b64(o, rs, WS_CS + grp * 1024 + l * 16 + (mi & 1) * 8, 0, 16);
  }
  read_frags<0, 0>(g, F1, xf);

  u32 gsrc;
  {
    const int grp = group_of(w);
    gsrc = (w < 5 ? WS_CS + grp * 1024 : WS_ST + mi * 2048 + (w == 5 ? 0 : 1024)) + l * 16;
  }
  u32x4 gat, pol;

#define STAGE(S, VS, VMW, PRE, POST)                 \
  read_frags<S, 1>(g, F2, xf);                       \
  sb(); do_mfma<S, 0, VS>(F1, xf, acc); sb();        \
  if constexpr (VMW >= 0) wait_vm<(VMW >= 0 ? VMW : 0)>(); \
  PRE;                                               \
  bar();                                             \
  POST;                                              \
  if constexpr (S < 19) read_frags<(S < 19 ? S + 1 : 0), 0>(g, F1, xf); \
  sb(); do_mfma<S, 1, VS>(F2, xf, acc); sb();

  auto gather_consume = [&]() {
    asm volatile("" : "+v"(gat));
#pragma unroll 1
    for (int it = 0; it < SPIN; it++) {
      if (__all(gat.y == T && gat.w == T)) break;
      __builtin_amdgcn_s_sleep(2);
      gat = ld_sc1_wait(gsrc, ws);
    }
    float a = asf(gat.x), c = asf(gat.z);
    if (w < 5) {
      u32 i = w == 2 ? 128 + l : (w < 2 ? 0 : 192) + (l >> 4) * 32 + ((w == 1 || w == 4) ? 16 : 0) + (l & 15);
      lds_st32f(LB + L_CS + i * 4, a + c);
    } else if (w < 7) {
      u32 t = (w - 5) * 64 + l;
      lds_st32f(LB + L_MU + t * 4, a); lds_st32f(LB + L_R + t * 4, c); lds_st32f(LB + L_RM + t * 4, a * c);
    }
  };
#define UNIT1(VS)                                                                   \
  STAGE(0, VS, 10, , (issue_block<9>(g), issue_block<10>(g)))                        \
  STAGE(1, VS, 10, , (issue_block<11>(g), issue_block<12>(g)))                       \
  STAGE(2, VS, 10, , (issue_block<13>(g), issue_block<14>(g)))                       \
  STAGE(3, VS, 10, , (issue_block<15>(g), issue_block<16>(g), gat = ld_sc1_asm(gsrc, ws))) \
  STAGE(4, VS, 10, , (issue_block<17>(g), issue_block<18>(g)))                       \
  STAGE(5, VS, 10, , (issue_block<19>(g), issue_block<20>(g)))                       \
  STAGE(6, VS, 10, , (issue_block<21>(g), issue_block<22>(g)))                       \
  STAGE(7, VS, 12, gather_consume(), (issue_block<23>(g), issue_block<24>(g)))

  const int m32 = mi * 4 + m;
  float rr, mu;
  auto epi_gu = [&](u32 ub, int pair) {
    const float a = -LOG2E * rr, bb = LOG2E * rr * mu, rinv = __builtin_amdgcn_rcpf(rr);
#pragma unroll
    for (int f = 0; f < 2; f++) {
      const int fr = 2 * nh + f;
      f32x4 cs[4];
#pragma unroll
      for (int q = 0; q < 4; q++) cs[q] = lds128f(LB + L_CS + (ub + fr * 32 + q * 8 + 4 * h) * 4);
      float hv[8];
#pragma unroll
      for (int j = 0; j < 8; j++) {
        float e = __builtin_amdgcn_exp2f(fmaf(acc[f][j], a, bb * cs[j >> 2][j & 3]));
        float u = fmaf(-mu, cs[2 + (j >> 2)][j & 3], acc[f][j + 8]);
        hv[j] = u * __builtin_amdgcn_rcpf(fmaf(e, rinv, rinv));
      }
      u32x4 o = {pk(hv[0], hv[1]), pk(hv[2], hv[3]), pk(hv[4], hv[5]), pk(hv[6], hv[7])};
      __builtin_amdgcn_raw_buffer_store_b128(o, rs, (l * 16) + (WS_ACT + (m32 * 144 + 4 * pair + fr) * 1024), 0, 16);
    }
  };

  acc[0] = f32x16{}; acc[1] = f32x16{};
  if (isV) {
    UNIT1(true)
    rr = lds32f(LB + L_R + (32 * m + r32) * 4); mu = lds32f(LB + L_MU + (32 * m + r32) * 4);
#pragma unroll
    for (int f = 0; f < 2; f++) {
      const int fr = 2 * nh + f;
      float cs = lds32f(LB + L_CS + (fr * 32 + r32) * 4);
      u32 o[8];
#pragma unroll
      for (int q = 0; q < 4; q++) {
        f32x4 r4 = lds128f(LB + L_R + (32 * m + 8 * q + 4 * h) * 4), m4 = lds128f(LB + L_RM + (32 * m + 8 * q + 4 * h) * 4);
        float v[4];
#pragma unroll
        for (int i = 0; i < 4; i++) v[i] = fmaf(acc[f][4 * q + i], r4[i], -m4[i] * cs);
        o[2 * q] = pk(v[0], v[1]); o[2 * q + 1] = pk(v[2], v[3]);
      }
      const u32 head = (2 * vy + (r32 >> 4)) * 4 + fr;
      const u32 vo = head * 65536 + (h * 32 + (r32 & 15)) * 16;
      u32x4 o0 = {o[0], o[1], o[2], o[3]}, o1 = {o[4], o[5], o[6], o[7]};
      __builtin_amdgcn_raw_buffer_store_b128(o0, rs, (vo) + (WS_V + m32 * 1024), 0, 16);
      __builtin_amdgcn_raw_buffer_store_b128(o1, rs, (vo + 256) + (WS_V + m32 * 1024), 0, 16);
    }
  } else {
    UNIT1(false)
    rr = lds32f(LB + L_R + (32 * m + r32) * 4); mu = lds32f(LB + L_MU + (32 * m + r32) * 4);
    epi_gu(0, p1);
  }
  STAMP(2);
  acc[0] = f32x16{};
  STAGE(8, false, 14, , issue_block<25>(g))
  STAGE(9, false, 14, , issue_block<26>(g))
  STAGE(10, false, 14, , issue_block<27>(g))
  STAGE(11, false, 14, , issue_w2<0>(g))
  {
    const float sc = rr * (nj < 8 ? 0.25f * LOG2E : 1.f), bb = -sc * mu;
    f32x4 cs[4];
#pragma unroll
    for (int q = 0; q < 4; q++) cs[q] = lds128f(LB + L_CS + (128 + nh * 32 + q * 8 + 4 * h) * 4);
    u32 o[8];
#pragma unroll
    for (int j = 0; j < 8; j++)
      o[j] = pk(fmaf(acc[0][2 * j], sc, bb * cs[j >> 1][(2 * j) & 3]), fmaf(acc[0][2 * j + 1], sc, bb * cs[j >> 1][(2 * j + 1) & 3]));
    u32x4 o0 = {o[0], o[1], o[2], o[3]}, o1 = {o[4], o[5], o[6], o[7]};
    const u32 so = (u32)((nj * 4 + nh * 2) * 64 + m32) * 1024;
    __builtin_amdgcn_raw_buffer_store_b128(o0, rs, (l * 16) + (WS_Q + so), 0, 16);
    __builtin_amdgcn_raw_buffer_store_b128(o1, rs, (l * 16) + (WS_Q + so + 65536), 0, 16);
  }
  STAMP(3);
  acc[0] = f32x16{}; acc[1] = f32x16{};
  STAGE(12, false, 14, , issue_w2<1>(g))
  STAGE(13, false, 14, , )
  STAGE(14, false, 0, , ({ if (threadIdx.x == 0) __builtin_amdgcn_raw_buffer_store_b32(T, rs, WS_F1 + (mi * 16 + nj) * 4, 0, 16); issue_w2<2>(g); issue_w2<3>(g); }))
  STAGE(15, false, -1, , issue_w2<4>(g))
  STAGE(16, false, -1, , issue_w2<5>(g))
  STAGE(17, false, -1, , )
  STAGE(18, false, -1, , pol = ld_sc1_asm(l * 16, ws + WS_F1))
  STAGE(19, false, -1, , )
  STAMP(4);
  {
    const int ml = l >> 2;
    const bool need = b == 0 || (ml <= mia && ml + 2 >= mia) || ml == mi2;
    asm volatile("s_waitcnt vmcnt(0)" : "+v"(pol)::"memory");
#pragma unroll 1
    for (int it = 0; it < SPIN; it++) {
      if (__all(!need || (pol.x == T && pol.y == T && pol.z == T && pol.w == T))) break;
      __builtin_amdgcn_s_sleep(2);
      pol = ld_sc1_wait(l * 16, ws + WS_F1);
    }
    if (b == 0 && threadIdx.x == 0) __builtin_amdgcn_raw_buffer_store_b32(T, rs, WS_EP, 0, 16);
  }
  STAMP(5);
  const int qt = w & 3, hd = w >> 2, head = 2 * hp + hd, TQ = mia * 4 + qt;
  u32x4 kv[6], qf;
  {
    const int ty = w >> 2, sh = 2 * hp + ((w >> 1) & 1), t0 = mia * 4 - 8 + (w & 1) * 6;
#pragma unroll
    for (int i = 0; i < 6; i++) {
      int t = t0 + i; t = t < 0 ? 0 : t;
      kv[i] = __builtin_amdgcn_raw_buffer_load_b128(rs, l * 16, WS_K + ty * (2u << 20) + (sh * 64 + t) * 1024, 16);
    }
    qf = __builtin_amdgcn_raw_buffer_load_b128(rs, l * 16, WS_Q + (head * 64 + TQ) * 1024, 16);
  }
  epi_gu(192, nj);
  {
    const int ty = w >> 2, t0 = mia * 4 - 8 + (w & 1) * 6;
#pragma unroll
    for (int i = 0; i < 6; i++) {
      u32x4 v = kv[i];
      if (ty == 1 && t0 + i < 0) v = u32x4{0, 0, 0, 0};
      lds_st128(LB + L_KV + (w * 6 + i) * 1024 + l * 16, v);
    }
    if (threadIdx.x < 384) {
      const int t = threadIdx.x >> 5;
      const u32 one = (mia * 4 - 8 + t >= 0) ? 0x3f803f80u : 0u;
      lds_st128(LB + L_ONE + threadIdx.x * 16, u32x4{one, one, one, one});
    }
  }
  u32x4 wA[6][2], wB[6][2];
  u32 aW;
  {
    u32 gq = l >> 4, r = (l >> 2) & 3, c = l & 3;
    aW = LB + w * 2048 + (4 * h + r) * 128 + (((r >> 1) & 1) << 6) + (gq & 1) * 32 + c * 8;
  }
#pragma unroll
  for (int i = 0; i < 6; i++) {
    wA[i][0] = trr2(aW + i * SLOT, aW + i * SLOT + 1024);
    wA[i][1] = trr2((aW ^ 64u) + i * SLOT, (aW ^ 64u) + i * SLOT + 1024);
  }
  bar();
  issue_w2<6>(g); issue_w2<7>(g); issue_w2<8>(g); issue_w2<9>(g); issue_w2<10>(g); issue_w2<11>(g);
  STAMP(6);
  {
    u32x4 kf[9];
    const u32 ka = LB + L_KV + (hd * 12 + qt) * 1024 + l * 16;
#pragma unroll
    for (int i = 0; i < 9; i++) kf[i] = lds128(ka + i * 1024);
    const f32x16 zero = {};
    f32x16 sd = mfma(kf[8], qf, zero), sf = mfma(kf[0], qf, zero);
    if (TQ < 8) {
#pragma unroll
      for (int i = 0; i < 16; i++) sf[i] = -__builtin_inff();
    }
    f32x16 sm;
    float mx = -__builtin_inff();
#pragma unroll
    for (int i = 0; i < 16; i++) {
      const u32 kk = (i & 3) + 8 * (i >> 2) + 4 * h;
      sm[i] = kk <= r32 ? sd[i] : sf[i];
    }
#pragma unroll
    for (int i = 0; i < 16; i += 2) mx = fmaxf(fmaxf(mx, sm[i]), sm[i + 1]);
#pragma unroll
    for (int t = 1; t < 8; t++) {
      f32x16 s = mfma(kf[t], qf, zero);
      float mn = mx;
#pragma unroll
      for (int i = 0; i < 16; i += 2) mn = fmaxf(fmaxf(mn, s[i]), s[i + 1]);
      mx = (TQ - 8 + t >= 0) ? mn : mx;
    }
    mx = xmax32(mx);
    wait_vm<0>();
    if (l == 0) {
      u32 old = __hip_atomic_fetch_add((LAS u32*)(size_t)(LB + L_CNT), 1u, __ATOMIC_RELAXED, __HIP_MEMORY_SCOPE_WORKGROUP);
      if (old == 7) __builtin_amdgcn_raw_buffer_store_b32(T, rs, WS_F2 + (mi * 16 + nj) * 4, 0, 16);
    }
    STAMP(7);
    f32x16 nm;
#pragma unroll
    for (int i = 0; i < 16; i++) nm[i] = -mx;
    const bool vl = r32 < 16;
    const u32 vst = vl ? 1024u : 512u;
    u32 va = vl ? LB + L_KV + 24576 + (hd * 12 + qt) * 1024 + h * 512 + r32 * 16 : LB + L_ONE + qt * 512 + (r32 - 16) * 16;
    f32x16 oa = {};
    {
      u32 p[8], pd[8], pf[8];
#pragma unroll
      for (int j = 0; j < 8; j++) {
        p[j] = pk(exp2c(sm[2 * j] - mx), exp2c(sm[2 * j + 1] - mx));
        const u32 k0 = ((2 * j) & 3) + 8 * (j >> 1) + 4 * h;
        const u32 mk = (k0 <= r32 ? 0xffffu : 0u) | (k0 + 1 <= r32 ? 0xffff0000u : 0u);
        pd[j] = p[j] & mk; pf[j] = p[j] ^ pd[j];
      }
      oa = mfma(lds128(va), u32x4{pf[0], pf[1], pf[2], pf[3]}, oa);
      oa = mfma(lds128(va + 256), u32x4{pf[4], pf[5], pf[6], pf[7]}, oa);
      u32 v8 = va + 8 * vst;
      oa = mfma(lds128(v8), u32x4{pd[0], pd[1], pd[2], pd[3]}, oa);
      oa = mfma(lds128(v8 + 256), u32x4{pd[4], pd[5], pd[6], pd[7]}, oa);
    }
#pragma unroll
    for (int t = 1; t < 8; t++) {
      f32x16 s = mfma(kf[t], qf, nm);
      va += vst;
      u32 p[8];
#pragma unroll
      for (int j = 0; j < 8; j++) p[j] = pk(exp2c(s[2 * j]), exp2c(s[2 * j + 1]));
      oa = mfma(lds128(va), u32x4{p[0], p[1], p[2], p[3]}, oa);
      oa = mfma(lds128(va + 256), u32x4{p[4], p[5], p[6], p[7]}, oa);
    }
    const float il = __builtin_amdgcn_rcpf(oa[8]);
    u32x4 o = {pk(oa[0] * il, oa[1] * il), pk(oa[2] * il, oa[3] * il), pk(oa[4] * il, oa[5] * il), pk(oa[6] * il, oa[7] * il)};
    __builtin_amdgcn_raw_buffer_store_b128(o, rs, (l * 16) + (WS_ACT + (TQ * 144 + 112 + head) * 1024), 0, 16);
  }
#pragma unroll
  for (int i = 0; i < 6; i++) {
    wB[i][0] = trr2(aW + i * SLOT, aW + i * SLOT + 1024);
    wB[i][1] = trr2((aW ^ 64u) + i * SLOT, (aW ^ 64u) + i * SLOT + 1024);
  }
  asm volatile("s_waitcnt vmcnt(0) lgkmcnt(0)" ::: "memory");
  if (l == 0) {
    u32 old = __hip_atomic_fetch_add((LAS u32*)(size_t)(LB + L_CNT + 4), 1u, __ATOMIC_RELAXED, __HIP_MEMORY_SCOPE_WORKGROUP);
    if (old == 7) __builtin_amdgcn_raw_buffer_store_b32(T, rs, WS_FO + (mia * 16 + hp) * 4, 0, 16);
  }
  STAMP(8);
  f32x16 c2[2][2] = {};
  const u32 abase = WS_ACT + (u32)(m64 * 2) * 144 * 1024;
  auto lda = [&](int mt, int ks) { return __builtin_amdgcn_raw_buffer_load_b128(rs, l * 16, abase + (mt * 144 + ks) * 1024, 16); };
  auto poll2 = [&](u32 off) {
#pragma unroll 1
    for (int it = 0; it < SPIN; it++) {
      u64 v = __hip_atomic_load((GAS u64*)(ws + off), __ATOMIC_RELAXED, __HIP_MEMORY_SCOPE_AGENT);
      if (__builtin_amdgcn_readfirstlane((u32)v) == T && __builtin_amdgcn_readfirstlane((u32)(v >> 32)) == T) break;
      __builtin_amdgcn_s_sleep(2);
    }
    asm volatile("" ::: "memory");
  };
  poll2(WS_F2 + (mi2 * 16 + 2 * w) * 4);
  issue_w2<12>(g); issue_w2<13>(g); issue_w2<14>(g); issue_w2<15>(g); issue_w2<16>(g); issue_w2<17>(g);
  {
    u32x4 a[6][2];
#pragma unroll
    for (int i = 0; i < 6; i++) { a[i][0] = lda(0, 64 + 6 * w + i); a[i][1] = lda(1, 64 + 6 * w + i); }
    u32x4 a2[8][2];
#pragma unroll
    for (int i = 0; i < 6; i++) {
      a2[i][0] = lda(0, 8 * w + i); a2[i][1] = lda(1, 8 * w + i);
#pragma unroll
      for (int mt = 0; mt < 2; mt++)
#pragma unroll
        for (int nt = 0; nt < 2; nt++) c2[mt][nt] = mfma(a[i][mt], wA[i][nt], c2[mt][nt]);
    }
    a2[6][0] = lda(0, 8 * w + 6); a2[6][1] = lda(1, 8 * w + 6);
    a2[7][0] = lda(0, 8 * w + 7); a2[7][1] = lda(1, 8 * w + 7);
#pragma unroll
    for (int i = 0; i < 6; i++)
#pragma unroll
      for (int mt = 0; mt < 2; mt++)
#pragma unroll
        for (int nt = 0; nt < 2; nt++) c2[mt][nt] = mfma(a2[i][mt], wB[i][nt], c2[mt][nt]);
    wait_vm<4>();
#pragma unroll
    for (int i = 0; i < 6; i++) {
      wA[i][0] = trr2(aW + i * SLOT, aW + i * SLOT + 1024);
      wA[i][1] = trr2((aW ^ 64u) + i * SLOT, (aW ^ 64u) + i * SLOT + 1024);
    }
#pragma unroll
    for (int i = 0; i < 2; i++)
#pragma unroll
      for (int mt = 0; mt < 2; mt++)
#pragma unroll
        for (int nt = 0; nt < 2; nt++) c2[mt][nt] = mfma(a2[6 + i][mt], wA[i][nt], c2[mt][nt]);
  }
  STAMP(9);
  poll2(WS_FO + (mi2 * 16 + 2 * w) * 4);
  STAMP(10);
  const u32 uu = threadIdx.x >> 3, oc = threadIdx.x & 7;
  const u32 tok = (uu & 1) * 4 + ((uu >> 1) & 3) + (uu >> 3) * 8;
  const size_t ooff = (size_t)(m64 * 64 + tok) * 1024 + n64 * 128 + oc * 16;
  u32x4 xres = *(const GAS u32x4*)(x + ooff);
  {
    u32x4 a[4][2];
#pragma unroll
    for (int i = 0; i < 4; i++) { a[i][0] = lda(0, 112 + 4 * w + i); a[i][1] = lda(1, 112 + 4 * w + i); }
#pragma unroll
    for (int i = 0; i < 4; i++)
#pragma unroll
      for (int mt = 0; mt < 2; mt++)
#pragma unroll
        for (int nt = 0; nt < 2; nt++) c2[mt][nt] = mfma(a[i][mt], wA[2 + i][nt], c2[mt][nt]);
  }
  bar();
#pragma unroll
  for (int mt = 0; mt < 2; mt++)
#pragma unroll
    for (int nt = 0; nt < 2; nt++)
#pragma unroll
      for (int i = 0; i < 16; i++) lds_st32f(LB + w * 16384 + (mt * 2 + nt) * 4096 + i * 256 + l * 4, c2[mt][nt][i]);
  bar();
  {
    const u32 t5 = tok & 31, reg = (t5 & 3) + 4 * (t5 >> 3), hh = (t5 >> 2) & 1;
    const u32 sw = (uu >> 1) & 1;
    const u32 ra = LB + ((tok >> 5) * 2 + (oc >> 2)) * 4096 + reg * 256 + hh * 128 + (oc & 3) * 32;
    f32x4 s0 = {}, s1 = {};
#pragma unroll
    for (int i = 0; i < 8; i++) { s0 += lds128f(ra + i * 16384 + sw * 16); s1 += lds128f(ra + i * 16384 + (sw ^ 1) * 16); }
    f32x4 lo = sw ? s1 : s0, hi = sw ? s0 : s1;
    u32x4 o = {pk(lo[0] + bflo(xres.x), lo[1] + bfhi(xres.x)), pk(lo[2] + bflo(xres.y), lo[3] + bfhi(xres.y)),
               pk(hi[0] + bflo(xres.z), hi[1] + bfhi(xres.z)), pk(hi[2] + bflo(xres.w), hi[3] + bfhi(xres.w))};
    *(GAS u32x4*)(out + ooff) = o;
  }
  STAMP(11);
  u64 cyc1 = __builtin_amdgcn_s_memtime();
  if (threadIdx.x == 0) {
#pragma unroll
    for (int i = 0; i < NSTAMP; i++) ((GAS u64*)(ws + WS_DBG))[b * 16 + i] = stamp[i];
    ((GAS u64*)(ws + WS_DBG))[b * 16 + 15] = cyc1 - cyc0;
    if (b == 77) {
      u64 d = PROBE_A >= 0 ? (stamp[PROBE_B] - stamp[PROBE_A >= 0 ? PROBE_A : 0]) * 10 : (PROBE_A == -1 ? 0 : (cyc1 - cyc0) * 100 / (stamp[11] - stamp[0]));
      while (__builtin_amdgcn_s_memrealtime() < stamp[0] + 10000 + d) __builtin_amdgcn_s_sleep(1);
    }
  }
}

extern "C" void kernel_launch(void* const* d_in, const int* in_sizes, int n_in, void* d_out, int out_size, void* d_ws,
                              size_t ws_size, hipStream_t stream) {
  fused_block<<<256, 512, 0, stream>>>((const char*)d_in[0], (const char*)d_in[1], (const char*)d_in[2], (const char*)d_in[4],
                                       (const char*)d_in[5], (const char*)d_in[6], (char*)d_out, (char*)d_ws);
}
```

```cpp
#include <hip/hip_runtime.h>

#define OE 7
#define C4L 1
typedef unsigned int u32;
typedef unsigned long long u64;
typedef u32 u32x4 __attribute__((ext_vector_type(4)));
typedef u32 u32x2 __attribute__((ext_vector_type(2)));
typedef float f32x2 __attribute__((ext_vector_type(2)));
typedef float f32x4 __attribute__((ext_vector_type(4)));
typedef float f32x16 __attribute__((ext_vector_type(16)));
typedef __bf16 bf16x8 __attribute__((ext_vector_type(8)));
typedef __bf16 bf16x2 __attribute__((ext_vector_type(2)));
typedef u32 u32x16 __attribute__((ext_vector_type(16)));
typedef short s16x4 __attribute__((ext_vector_type(4)));
#define LAS __attribute__((address_space(3)))
#define GAS __attribute__((address_space(1)))
#define DI __device__ __forceinline__
#define BC(T, v) __builtin_bit_cast(T, v)

constexpr u32 WS_Q = 0, WS_K = 2u << 20, WS_V = 4u << 20, WS_ACT = 6u << 20;
constexpr u32 WS_ST = 15u << 20;
constexpr u32 WS_CS = WS_ST + 32768;
constexpr u32 FS = 128;
constexpr u32 WS_F1 = WS_ST + 393216;
constexpr u32 WS_F2 = WS_F1 + 256 * FS;
constexpr u32 WS_EP = WS_F2 + 256 * FS;
constexpr u32 WS_END = WS_EP + 256;
constexpr u32 WS_CP = 20u << 20;
constexpr u32 WS_DBG = 16u << 20;
constexpr int NSTAMP = 16;

constexpr u32 SLOT = 16384;
constexpr u32 MISC = 9 * SLOT;
constexpr u32 L_BD = MISC, L_BF = MISC + 4096, L_MK = MISC + 8192;
constexpr u32 L_ONE = MISC + 10240;
constexpr u32 L_CNT = L_ONE + 2032;
constexpr u32 L_CS = MISC + 12288;
constexpr u32 L_PF = MISC + 13824;
constexpr u32 L_PAD = MISC + 14336;
constexpr u32 L_KV = 4 * SLOT;
constexpr u32 LDS_BYTES = 163840;

constexpr float LOG2E = 1.4426950408889634f;
constexpr int SPIN = 20000;
constexpr u32 OOB = 0x7f000000u;

DI float asf(u32 v) { return __builtin_bit_cast(float, v); }
DI u32 asu(float v) { return __builtin_bit_cast(u32, v); }
DI f32x16 mfma(u32x4 a, u32x4 b, f32x16 c) {
  return __builtin_amdgcn_mfma_f32_32x32x16_bf16(BC(bf16x8, a), BC(bf16x8, b), c, 0, 0, 0);
}
DI f32x4 mfma16(u32x4 a, u32x4 b, f32x4 c) {
  return __builtin_amdgcn_mfma_f32_16x16x32_bf16(BC(bf16x8, a), BC(bf16x8, b), c, 0, 0, 0);
}
DI u32x2 trr(u32 addr) { return BC(u32x2, __builtin_amdgcn_ds_read_tr16_b64_v4i16((LAS s16x4*)(size_t)addr)); }
DI u32x4 trr2(u32 a0, u32 a1) { u32x2 lo = trr(a0), hi = trr(a1); u32x4 r = {lo.x, lo.y, hi.x, hi.y}; return r; }
DI u32x4 lds128(u32 addr) { return *(LAS u32x4*)(size_t)addr; }
DI f32x4 lds128f(u32 addr) { return *(LAS f32x4*)(size_t)addr; }
DI float lds32f(u32 addr) { return *(LAS float*)(size_t)addr; }
DI void lds_st32f(u32 addr, float v) { *(LAS float*)(size_t)addr = v; }
DI void lds_st128(u32 addr, u32x4 v) { *(LAS u32x4*)(size_t)addr = v; }
DI u32 pk(float a, float b) { asm("" : "+v"(a)); asm("" : "+v"(b)); f32x2 v = {a, b}; return BC(u32, __builtin_convertvector(v, bf16x2)); }
DI float bflo(u32 v) { return BC(float, v << 16); }
DI float bfhi(u32 v) { return BC(float, v & 0xffff0000u); }
template <u32 O> DI void glds(u32 voff, const char* sbase, u32 lb) {
  asm volatile("s_add_u32 m0, %2, %3\n\ts_nop 0\n\tglobal_load_lds_dwordx4 %0, %1" ::"v"(voff), "s"(sbase), "s"(lb), "i"(O) : "memory", "scc");
}
template <u32 O, u32 X> DI void glds_x(u32& voff, const char* sbase, u32 lb) {
  asm volatile("s_add_u32 m0, %2, %3\n\tv_xor_b32 %0, %4, %0\n\tglobal_load_lds_dwordx4 %0, %1" : "+v"(voff) : "s"(sbase), "s"(lb), "i"(O), "i"(X) : "memory", "scc");
}
template <u32 O, u32 X> DI void glds_t(u32 voff, const char* sbase, u32 lb) {
  u32 t;
  asm volatile("s_add_u32 m0, %3, %4\n\tv_xor_b32 %0, %5, %1\n\tglobal_load_lds_dwordx4 %0, %2" : "=v"(t) : "v"(voff), "s"(sbase), "s"(lb), "i"(O), "i"(X) : "memory", "scc");
}
template <u32 O> DI void glds_xs(u32& voff, u32 x, const char* sbase, u32 lb) {
  asm volatile("s_add_u32 m0, %2, %3\n\tv_xor_b32 %0, %4, %0\n\tglobal_load_lds_dwordx4 %0, %1" : "+v"(voff) : "s"(sbase), "s"(lb), "i"(O), "s"(x) : "memory", "scc");
}
template <u32 O, bool ADD> DI void glds_d(u32& voff, int d, const char* sbase, u32 lb) {
  if constexpr (ADD) asm volatile("s_add_u32 m0, %2, %3\n\tv_add_u32 %0, %4, %0\n\tglobal_load_lds_dwordx4 %0, %1" : "+v"(voff) : "s"(sbase), "s"(lb), "i"(O), "s"(d) : "memory", "scc");
  else asm volatile("s_add_u32 m0, %2, %3\n\tv_subrev_u32 %0, %4, %0\n\tglobal_load_lds_dwordx4 %0, %1" : "+v"(voff) : "s"(sbase), "s"(lb), "i"(O), "s"(d) : "memory", "scc");
}
template <u32 O, u32 X> DI void blds1(u32 voff, __amdgpu_buffer_rsrc_t rs, u32 lo, u32 soff, u32 lb) {
  u32 t;
  asm volatile("s_add_u32 vcc_lo, %4, %7\n\ts_cmp_lt_u32 vcc_lo, %3\n\ts_cselect_b32 vcc_hi, 0x200, -1\n\ts_max_u32 vcc_lo, vcc_lo, %3\n\ts_add_u32 m0, %5, %6\n\t"
               "v_and_b32 %0, vcc_hi, %1\n\tbuffer_load_dwordx4 %0, %2, vcc_lo offen sc1 lds"
               : "=v"(t) : "v"(voff), "s"(rs), "s"(lo), "s"(soff), "s"(lb), "i"(O), "i"(X) : "memory", "scc", "vcc");
}
DI void st128(u32x4 v, __amdgpu_buffer_rsrc_t rs, u32 voff, u32 soff) {
  asm volatile("buffer_store_dwordx4 %0, %1, %2, %3 offen sc1\n\ts_nop 1" ::"v"(v), "v"(voff), "s"(rs), "s"(soff) : "memory");
}
DI u64 wmask(bool c) { return __builtin_amdgcn_ballot_w64(c); }
template <class A> DI A sel(bool c, A a, A b) { return c ? a : b; }
DI void glds_pf(u32 voff, const char* sbase, u32 ldsaddr, u64 em) {
  asm volatile("s_mov_b32 m0, %2\n\ts_mov_b64 exec, %3\n\tglobal_load_lds_dword %0, %1\n\ts_mov_b64 exec, -1" ::"v"(voff), "s"(sbase), "s"(ldsaddr), "s"(em) : "memory");
}
DI u32 bld1(u32 voff, __amdgpu_buffer_rsrc_t rs) {
  u32 r; asm volatile("buffer_load_dword %0, %1, %2, 0 offen sc1" : "=v"(r) : "v"(voff), "s"(rs) : "memory"); return r;
}
template <u32 O> DI u32x4 bld4(u32 voff, __amdgpu_buffer_rsrc_t rs, u32 soff) {
  u32x4 r;
  if constexpr (O == 0) asm volatile("buffer_load_dwordx4 %0, %1, %2, %3 offen sc1" : "=v"(r) : "v"(voff), "s"(rs), "s"(soff) : "memory");
  else asm volatile("s_add_u32 vcc_lo, %3, %4\n\tbuffer_load_dwordx4 %0, %1, %2, vcc_lo offen sc1" : "=v"(r) : "v"(voff), "s"(rs), "s"(soff), "i"(O) : "memory", "scc", "vcc");
  return r;
}
template <u32 O, u32 X> DI void blds_s(u32 voff, __amdgpu_buffer_rsrc_t rs, u32 soff, u32 lb, u32 sh) {
  asm volatile("s_add_u32 vcc_lo, %2, %5\n\ts_add_u32 m0, %3, %4\n\ts_lshr_b32 vcc_lo, vcc_lo, %6\n\tbuffer_load_dwordx4 %0, %1, vcc_lo offen sc1 lds"
               ::"v"(voff), "s"(rs), "s"(soff), "s"(lb), "i"(O), "i"(X), "s"(sh) : "memory", "scc", "vcc");
}
DI bool unset4(const u32x4& a, const u32x4& b, const u32x4& c, const u32x4& d) {
  u32 m = max(max(max(a.x, a.y), a.z), max(max(a.w, b.x), b.y)), n = max(max(max(b.z, b.w), c.x), max(max(c.y, c.z), c.w));
  m = max(max(m, d.x), d.y); n = max(max(n, d.z), d.w);
  return __builtin_amdgcn_ballot_w64(max(m, n) == 0xffffffffu) != 0;
}
template <int N> DI void wait4(u32x4& a, u32x4& b, u32x4& c, u32x4& d) {
  asm volatile("s_waitcnt vmcnt(%4)" : "+v"(a), "+v"(b), "+v"(c), "+v"(d) : "i"(N) : "memory");
}
DI void ew_m(float& a, float& b, float c) {
  float x, y;
  asm volatile("v_mul_f32 %0, %2, %3\n\tv_mul_f32 %1, %2, %4" : "=&v"(x), "=v"(y) : "v"(c), "v"(a), "v"(b));
  a = x; b = y;
}
DI void ew_a(float (&e)[4], float a0, float a1, float a2, float a3, float c) {
  asm volatile("v_fmac_f32 %0, %4, %8\n\tv_fmac_f32 %1, %5, %8\n\tv_fmac_f32 %2, %6, %8\n\tv_fmac_f32 %3, %7, %8"
               : "+v"(e[0]), "+v"(e[1]), "+v"(e[2]), "+v"(e[3]) : "v"(a0), "v"(a1), "v"(a2), "v"(a3), "v"(c));
}
DI void ew_b(float (&e)[4], float (&u)[4], float a0, float a1, float a2, float a3, float mu) {
  float x[4];
  asm volatile("v_exp_f32 %0, %0\n\tv_fma_f32 %4, -%12, %13, %8\n\tv_exp_f32 %1, %1\n\tv_fma_f32 %5, -%12, %14, %9\n\t"
               "v_exp_f32 %2, %2\n\tv_fma_f32 %6, -%12, %15, %10\n\tv_exp_f32 %3, %3\n\tv_fma_f32 %7, -%12, %16, %11"
               : "+v"(e[0]), "+v"(e[1]), "+v"(e[2]), "+v"(e[3]), "=&v"(x[0]), "=&v"(x[1]), "=&v"(x[2]), "=v"(x[3])
               : "v"(a0), "v"(a1), "v"(a2), "v"(a3), "v"(mu), "v"(u[0]), "v"(u[1]), "v"(u[2]), "v"(u[3]));
  u[0] = x[0]; u[1] = x[1]; u[2] = x[2]; u[3] = x[3];
}
DI u32x2 ew_c(float (&e)[4], const float (&u)[4], float c) {
  u32 r0, r1;
  asm volatile("v_fma_f32 %2, %2, %10, %10\n\tv_fma_f32 %3, %3, %10, %10\n\tv_fma_f32 %4, %4, %10, %10\n\tv_fma_f32 %5, %5, %10, %10\n\t"
               "v_rcp_f32 %2, %2\n\tv_rcp_f32 %3, %3\n\tv_rcp_f32 %4, %4\n\tv_rcp_f32 %5, %5\n\t"
               "v_mul_f32 %2, %2, %6\n\tv_mul_f32 %3, %3, %7\n\tv_mul_f32 %4, %4, %8\n\tv_mul_f32 %5, %5, %9\n\t"
               "v_cvt_pk_bf16_f32 %0, %2, %3\n\tv_cvt_pk_bf16_f32 %1, %4, %5"
               : "=&v"(r0), "=v"(r1), "+v"(e[0]), "+v"(e[1]), "+v"(e[2]), "+v"(e[3]) : "v"(u[0]), "v"(u[1]), "v"(u[2]), "v"(u[3]), "v"(c));
  return u32x2{r0, r1};
}
DI void wait8(u32x4 (&o)[8]) {
  asm volatile("s_waitcnt vmcnt(0)" : "+v"(o[0]), "+v"(o[1]), "+v"(o[2]), "+v"(o[3]), "+v"(o[4]), "+v"(o[5]), "+v"(o[6]), "+v"(o[7])::"memory");
}
DI void spin1(u32& p, u32 T, u32 voff, __amdgpu_buffer_rsrc_t rs) {
  u32 c;
  asm volatile("s_movk_i32 %1, %5\n1:\n\ts_sleep 2\n\tbuffer_load_dword %0, %3, %4, 0 offen sc1\n\ts_waitcnt vmcnt(0)\n\tv_cmp_ne_u32 vcc, %2, %0\n\t"
               "s_cbranch_vccz 2f\n\ts_sub_u32 %1, %1, 1\n\ts_cbranch_scc0 1b\n2:"
               : "+v"(p), "=&s"(c) : "v"(T), "v"(voff), "s"(rs), "i"(SPIN) : "memory", "vcc", "scc");
}
DI void check1(u32& p, u32 T, u32 voff, __amdgpu_buffer_rsrc_t rs) {
  const u32 t = voff < WS_END ? T : 0u;
  if (__builtin_expect(__builtin_amdgcn_ballot_w64(p != t) != 0, (OE >> 3) & 1)) spin1(p, t, voff, rs);
}
template <int N> DI void wait_vm() { static_assert(N >= 0, "order"); asm volatile("s_waitcnt vmcnt(%0)" ::"i"(N) : "memory"); }
DI void bar() { asm volatile("s_waitcnt lgkmcnt(0)\n\ts_barrier" ::: "memory"); }
DI void sb() { __builtin_amdgcn_sched_barrier(0); }
DI u32x4 ld_sc1_asm(u32 voff, const char* sbase) {
  u32x4 r; asm volatile("global_load_dwordx4 %0, %1, %2 sc1" : "=v"(r) : "v"(voff), "s"(sbase) : "memory"); return r;
}
DI void ld1_sc1_rw(u32& r, u32 voff, const char* sbase) {
  asm volatile("global_load_dword %0, %1, %2 sc1" : "+v"(r) : "v"(voff), "s"(sbase) : "memory");
}
DI u32 ld1_sc1(u32 voff, const char* sbase) {
  u32 r; asm volatile("global_load_dword %0, %1, %2 sc1" : "=v"(r) : "v"(voff), "s"(sbase) : "memory"); return r;
}
DI u32 ld1_sc1_wait(u32 voff, const char* sbase) {
  u32 r; asm volatile("global_load_dword %0, %1, %2 sc1\n\ts_waitcnt vmcnt(0)" : "=&v"(r) : "v"(voff), "s"(sbase) : "memory"); return r;
}
DI u32x4 ld_asm(u32 voff, const char* sbase) {
  u32x4 r; asm volatile("global_load_dwordx4 %0, %1, %2" : "=v"(r) : "v"(voff), "s"(sbase) : "memory"); return r;
}
template <int N, class A> DI void wait_vm(A& a) { static_assert(N >= 0, "order"); asm volatile("s_waitcnt vmcnt(%1)" : "+v"(a) : "i"(N) : "memory"); }
template <int N, class A, class B> DI void wait_vm(A& a, B& b) { static_assert(N >= 0, "order"); asm volatile("s_waitcnt vmcnt(%2)" : "+v"(a), "+v"(b) : "i"(N) : "memory"); }
template <class A> DI void tie(A& a) { asm volatile("" : "+v"(a)::"memory"); }
DI u32x4 ld_sc1_wait(u32 voff, const char* sbase) {
  u32x4 r; asm volatile("global_load_dwordx4 %0, %1, %2 sc1\n\ts_waitcnt vmcnt(0)" : "=&v"(r) : "v"(voff), "s"(sbase) : "memory"); return r;
}
DI float xsum32(float v) {
  u32x2 r = __builtin_amdgcn_permlane32_swap(asu(v), asu(v), false, false);
  return asf(r.x) + asf(r.y);
}
DI float xmax32(float v) {
  u32x2 r = __builtin_amdgcn_permlane32_swap(asu(v), asu(v), false, false);
  return fmaxf(asf(r.x), asf(r.y));
}
DI float wsum(float v) {
  v += BC(float, __builtin_amdgcn_update_dpp(0, BC(int, v), 0xB1, 0xf, 0xf, true));
  v += BC(float, __builtin_amdgcn_update_dpp(0, BC(int, v), 0x4E, 0xf, 0xf, true));
  v += BC(float, __builtin_amdgcn_update_dpp(0, BC(int, v), 0x141, 0xf, 0xf, true));
  v += BC(float, __builtin_amdgcn_update_dpp(0, BC(int, v), 0x140, 0xf, 0xf, true));
  u32x2 r = __builtin_amdgcn_permlane16_swap(asu(v), asu(v), false, false);
  v = asf(r.x) + asf(r.y);
  return xsum32(v);
}
DI float exp2c(float x) { return __builtin_amdgcn_fmed3f(__builtin_amdgcn_exp2f(x), 0.f, 1.f); }

DI void ep_a(float& e0, float& e1, float a0, float a1, float cA, float cB, float c0, float c1) {
  asm volatile("v_mul_f32 %0, %5, %6\n\tv_mul_f32 %1, %5, %7\n\tv_fmac_f32 %0, %2, %4\n\tv_fmac_f32 %1, %3, %4"
               : "=&v"(e0), "=&v"(e1) : "v"(a0), "v"(a1), "v"(cA), "v"(cB), "v"(c0), "v"(c1));
}
DI u32 ep_pk(float a, float b) { u32 r; asm volatile("v_cvt_pk_bf16_f32 %0, %1, %2" : "=v"(r) : "v"(a), "v"(b)); return r; }
DI void ep_b(float& e0, float& e1, float& u0, float& u1, float a0, float a1, float mu, float c0, float c1) {
  asm volatile("v_exp_f32 %0, %0\n\tv_fma_f32 %2, -%6, %7, %4\n\tv_exp_f32 %1, %1\n\tv_fma_f32 %3, -%6, %8, %5"
               : "+v"(e0), "+v"(e1), "=&v"(u0), "=v"(u1) : "v"(a0), "v"(a1), "v"(mu), "v"(c0), "v"(c1));
}
DI void ep_c(float& f0, float& f1, float e0, float e1, float c) {
  asm volatile("v_fma_f32 %0, %2, %4, %4\n\tv_fma_f32 %1, %3, %4, %4\n\tv_rcp_f32 %0, %0\n\tv_rcp_f32 %1, %1\n\ts_nop 0"
               : "=&v"(f0), "=&v"(f1) : "v"(e0), "v"(e1), "v"(c));
}
DI void ep_cm(float& f0, float& f1, float e0, float e1, float c, u64 em) {
  asm volatile("s_mov_b64 exec, %5\n\tv_fma_f32 %0, %2, %4, %4\n\tv_fma_f32 %1, %3, %4, %4\n\tv_rcp_f32 %0, %0\n\tv_rcp_f32 %1, %1\n\ts_mov_b64 exec, -1"
               : "+v"(f0), "+v"(f1) : "v"(e0), "v"(e1), "v"(c), "s"(em));
}
DI u32 ep_fin(float u0, float u1, float f0, float f1) {
  u32 r; float t;
  asm volatile("v_mul_f32 %0, %2, %4\n\tv_mul_f32 %1, %3, %5\n\tv_cvt_pk_bf16_f32 %0, %0, %1" : "=&v"(r), "=&v"(t) : "v"(u0), "v"(u1), "v"(f0), "v"(f1));
  return r;
}
#define SW 0
DI void check4(u32x4& p, u32 T, u32 voff, const char* ws, __amdgpu_buffer_rsrc_t rs) {
  if (__builtin_expect(__builtin_amdgcn_ballot_w64(p.y != T || p.w != T) != 0, (OE >> 4) & 1)) {
#pragma unroll 1
    for (int it = 0; it < SPIN; it++) {
      p = ld_sc1_wait(voff, ws);
      if (__builtin_amdgcn_ballot_w64((p.y ^ T) | (p.w ^ T)) == 0) break;
    }
  }
}
DI u32 cnt_add(u32 a, u32 one) {
  u32 r = 0;
  asm volatile("s_mov_b64 exec, 1\n\tds_add_rtn_u32 %0, %1, %2\n\ts_mov_b64 exec, -1" : "+v"(r) : "v"(a), "v"(one) : "memory");
  return r;
}
DI void cnt_add0(u32 a, u32 one) {
  asm volatile("s_mov_b64 exec, 1\n\tds_add_u32 %0, %1\n\ts_mov_b64 exec, -1" ::"v"(a), "v"(one) : "memory");
}
template <int O> DI u32x4 ld_sc1_off(u32 voff, const char* sbase) {
  u32x4 r; asm volatile("global_load_dwordx4 %0, %1, %2 offset:%3 sc1" : "=v"(r) : "v"(voff), "s"(sbase), "i"(O) : "memory"); return r;
}
DI u32 umax3(u32 a, u32 b, u32 c) { return max(max(a, b), c); }
DI bool unset(const u32x4 (&o)[8]) {
  u32 m = max(o[0].x, o[0].y);
  m = umax3(m, o[0].z, o[0].w);
  u32 n = max(o[1].x, o[1].y);
  n = umax3(n, o[1].z, o[1].w);
#pragma unroll
  for (int i = 2; i < 8; i += 2) { m = umax3(m, o[i].x, o[i].y); n = umax3(n, o[i + 1].x, o[i + 1].y); m = umax3(m, o[i].z, o[i].w); n = umax3(n, o[i + 1].z, o[i + 1].w); }
  return __builtin_amdgcn_ballot_w64(max(m, n) == 0xffffffffu) != 0;
}
DI const char* ptr_of(const u32x16& k, int i) { return (const char*)(((u64)k[2 * i + 1] << 32) | k[2 * i]); }
template <class A> DI void ties(A& a) { asm volatile("" : "+s"(a)); }

constexpr int WSL[20] = {1, 3, 5, 7, 1, 3, 5, 7, 8, 0, 1, 2, 3, 4, 5, 6, 7, 8, 0, 1};
constexpr int RS[2][5] = {{1, 4, 5, 6, 0}, {2, 3, 7, 8, 0}};
constexpr u32 xslot(int s) { return (u32)(2 * (s & 3)) * SLOT; }
constexpr u32 wslot(int s) { return (u32)WSL[s] * SLOT; }
constexpr u32 rpos(int r, int i) { return (u32)RS[r][(i % 10) >> 1] * SLOT + (i & 1) * 2048; }
constexpr int FLIP[8] = {0, 0, 1, 0, 2, 0, 1, 0};
constexpr bool UP[8] = {true, true, true, false, true, true, false, false};

constexpr int barG(int k) { return 8 * k + 3; }
constexpr bool has_bar(int k) { return k < 8 || (k & 3) == 1; }
constexpr int next_bar(int G) { for (int k = 0; k < 20; k++) if (has_bar(k) && barG(k) >= G) return barG(k); return -1; }
#define TQE 1
#define GV1 0
#define GV2 1
#define G3V 128
#define MV 0
#define VSK 0
#define WM 0
#define POL1 146
#define POL0 134
#define F1G 110
#define KVD 2
constexpr int CS1 = 16, CS3 = 78, GA = 52, GC = GV1 ? GV1 : 66, G3 = G3V, GS = 62, EP1 = 64, QKE = 97;
static_assert(GA + 7 <= barG(7) && GC + 7 <= barG(9) && G3 + 7 <= barG(17), "visible");
constexpr int PA(int p, int g = 0) { return 1000 + 8 * p + g; }
constexpr int PG(int p, int g = 0) { return 2000 + 8 * p + g; }
constexpr int SG_(int i, int g) { return PG(4 + i, g); }
#define GPUB 1
#define APRIO 2
#define GPRIO 1
#define GSV 0
#define ODLY 0
#define CPL 29
#define CPS 45
#define CPB 2
#define OPRIO 0
#define OPRIO2 2
#define OPRIO3 2
#define OFIRST 0
#define EPRIO 1
#define APF 0
#define GD 99
#define GU 99
#define GSIG 90
#define E3R 141
#define E3M 151
#define CPD 3
#define APUB 1
#define OODD 0
#define LPOL 8
#define LCHK 11
constexpr int NS = 8;
template <int N> struct IC { static constexpr int v = N; };

#define PFT 5
#define PF2 0
#define PF3 0
#define PF3D 10
#define PF3N 9
#define PF3W 1
#define C3T 59
#define C3D 3
#define C3W 2
#define PFM 3
#define CPF 4
#define XPF 0
#define XPW 1
#define AP1 0
#define AP2 0
#define CBAR 0
#define EAPRIO 0
#define EGV 0
#define LIG -1
#define PFW 2
enum { OB, OEV, OXR, OGAT, OST, OW2, OPOL, OKV, OQ, OWG, OAG, OWO, ONOP, OWS, OOA, ORES, OSV, OSX, OSEN, OEP };
struct Op { int t, kind, arg; };
#define S4(t, s) {t, OB, 4 * (s)}, {t + 2, OB, 4 * (s) + 1}, {t + 4, OB, 4 * (s) + 2}, {t + 7, OB, 4 * (s) + 3}
#define PR(t, k, i) {t, k, 2 * (i)}, {t, k, 2 * (i) + 1}
constexpr Op OPS[] = {
    {-200, OB, 0}, {-200, OB, 1}, {-200, OB, 2}, {-200, OB, 3},
    {-100, OEV, 0}, {-100, OXR, 0}, {-100, OB, 4}, {-100, OB, 5}, {-100, OB, 6}, {-100, OB, 7},
    {0, OB, 8}, {1, OB, 9}, {2, OB, 10}, {3, OB, 11},
    {4, OB, 32},
    {6, OB, 12}, {8, OB, 13}, {10, OB, 14}, {11, OB, 15},
    S4(12, 4),
    {20, OB, 20}, {22, OB, 21}, {24, OB, 22}, {26, OB, 23}, {27, OSX, 0}, {27, OSX, 1},
    S4(28, 6), S4(36, 7),
    {43, OGAT, 0}, {44, OB, 33}, {46, OB, 34}, {47, GV1 ? OGAT : ONOP, 1}, {48, OB, 35}, {50, OB, 36}, {51, OGAT, 3},
    {53, OB, 37}, {55, OB, 38}, {56, GV1 ? ONOP : OGAT, 1}, {58, OB, 39},
    {60, OB, 40}, {62, OB, 41}, {64, OB, 42}, {66, OB, 43}, {67, OSEN, 0}, {70, OB, 44}, {73, OB, 45}, {76, OB, 46}, {78, OST, 0}, {78, OSV, 0}, {79, OB, 47}, {82, OB, 48}, {84, OSX, 2}, {85, OB, 49}, {88, OB, 50}, {90, OST, 1}, {90, OSV, 1}, {92, OB, 51}, {99, OST, 2}, {101, OST, 3},
    {104, GV2 == 1 ? OGAT : ONOP, 2},
    {108, OB, 52}, {109, OB, 53}, {111, GV2 == 2 ? OGAT : ONOP, 2}, {112, OSX, 3}, {113, OWS, 0}, {115, OB, 54}, {116, OWS, 1}, {117, OB, 55}, {118, GV2 ? ONOP : OGAT, 2}, {119, OWS, 2}, {121, OW2, 0},
    {122, OWS, 3}, {124, OW2, 1}, {125, OWS, 4}, {127, OW2, 2}, {129, OWS, 5}, {131, OW2, 3}, {132, OWS, 6}, {134, OSX, 4}, {136, OWS, 7}, {140, OW2, 4},
    {142, OW2, 5}, {146, OKV, 0}, {148, OKV, 1}, {150, OKV, 2}, {152, OKV, 3}, {154, OKV, 4}, {156, OKV, 5},
    {PA(0, 0), OQ, 0}, {PA(0, 1), OQ, 1},
    {PA(1, 0), OST, 4},
    {PA(2, 0), OST, 5}, {PA(2, 1), ORES, 0}, {PA(3, 0), OSX, 5}, {PA(3, 1), OST, 6},
    PR(PA(5), OOA, 0), PR(PA(5), OOA, 1), PR(PA(5), OOA, 2), PR(PA(5), OOA, 3),
    {PG(0, 0), OAG, 0}, {PG(0, 0), OAG, 1}, {PG(1, 0), OST, 4}, {PG(2, 0), OST, 5}, {PG(2, 0), OAG, 2}, {PG(2, 0), OAG, 3}, {PG(3, 0), OAG, 4}, {PG(3, 0), OAG, 5}, {PG(3, 0), OAG, 6}, {PG(3, 0), OAG, 7},
#define GW_(i, g, n) PR(SG_(i, g), OWG, n)
#define GA_(i, g, n) PR(SG_(i, g), OAG, n)
#define GX_(i) {SG_(i, 0), i == GPUB ? OSX : (i == LPOL ? OPOL : ONOP), i == GPUB ? 5 : 0}
    GX_(0), GA_(0, 1, 4), GW_(0, 2, 10), GA_(0, 3, 5), GX_(1), GA_(1, 1, 6), GW_(1, 2, 11), GA_(1, 3, 7), GX_(2), GA_(2, 1, 8), GW_(2, 2, 12), GA_(2, 3, 9),
    GX_(3), GA_(3, 1, 10), GW_(3, 2, 13), GA_(3, 3, 11), GX_(4), GW_(4, 2, 14), {SG_(4, 4), ORES, 0}, GX_(5), GW_(5, 2, 15), GX_(6), GW_(6, 2, 16), GX_(7),
    GW_(7, 2, 17), GX_(8), GW_(8, 2, 18), GX_(9), GW_(9, 2, 19), GX_(10), GW_(10, 2, 20), GA_(11, 1, 12), GA_(11, 1, 13), GA_(11, 2, 14), GA_(11, 2, 15),
    GA_(11, 3, 16), GA_(11, 3, 17), GA_(11, 4, 18), GA_(11, 4, 19), GW_(11, 4, 21), GA_(12, 1, 20), GW_(12, 2, 22), GA_(12, 3, 21), GW_(13, 2, 23),
    GA_(13, 3, 22), GW_(14, 2, 24), GA_(14, 3, 23), GW_(15, 2, 25), {SG_(15, 3), OAG, 49}, {SG_(15, 3), OAG, 51}, GW_(16, 2, 26), {SG_(16, 3), OAG, 53},
    {SG_(16, 3), OAG, 55}, GW_(17, 2, 27), {SG_(17, 3), OAG, 57}, {SG_(17, 3), OAG, 59}, {SG_(18, 1), OAG, 61}, {SG_(18, 1), OAG, 63}, GW_(18, 3, 28),
    GW_(19, 3, 29), {SG_(20, 1), OAG, 48}, {SG_(20, 1), OAG, 50}, GW_(20, 3, 30), {SG_(21, 1), OAG, 52}, GW_(21, 3, 31), {SG_(21, 4), OAG, 54},
    {SG_(22, 1), OAG, 56}, {SG_(22, 2), OAG, 58}, {SG_(22, 3), OAG, 60}, {SG_(22, 4), OAG, 62},
};
constexpr int NOPS = sizeof(OPS) / sizeof(Op);
constexpr bool ins(int i, int t) { return OPS[i].t < 1000 || (OPS[i].t >= 2000) == (t >= 2000); }
constexpr int op_find(int kind, int arg, int t = 0) { for (int i = 0; i < NOPS; i++) if (OPS[i].kind == kind && OPS[i].arg == arg && ins(i, t)) return i; return -1000; }
constexpr int op_upto_t(int t) { int n = 0; for (int i = 0; i < NOPS; i++) if (OPS[i].t <= t) n++; return n; }
constexpr int op_time(int kind, int arg) { return OPS[op_find(kind, arg)].t; }
constexpr int counted(int i) { return OPS[i].kind != ONOP && !(VSK && OPS[i].kind == OSV); }
constexpr int cnt_range(int a, int e, int t) { int n = 0; for (int i = a; i < e; i++) if (ins(i, t)) n += counted(i); return n > 63 ? 63 : n; }
constexpr int vm_t(int kind, int arg, int t) { int f = op_find(kind, arg, t), e = op_upto_t(t); return (f < 0 || e <= f) ? -1 : cnt_range(f + 1, e, t); }
constexpr int vm_h(int kind, int arg, int t) { int n = vm_t(kind, arg, t); for (int i = 0; i < NOPS; i++) if (OPS[i].t == t && OPS[i].kind == OSX) n--; return n; }
constexpr int imin(int a, int b) { return a < b ? a : b; }
constexpr int free_t(const Op& o) {
  constexpr int XS = 100;
  int prev = -1;
  if (o.kind == OB) {
    constexpr int F[12] = {-1, XS + 4, 4, XS + 5, 5, XS + 6, 6, XS + 7, 7, 8, 9, 10};
    prev = o.arg < 16 ? -1 : (o.arg < 32 ? o.arg / 4 - 4 : F[(o.arg - 32) >> 1]);
  }
  if (o.kind == OW2) prev = o.arg < 4 ? 11 + (o.arg >> 1) : 16;
  if (o.kind == OWG) return SG_((o.arg >> 1) - 10, 1);
  if (o.kind == OWO) return PA(3, 1);
  if (o.kind == OAG && o.arg >= 24) return SG_(LCHK, 0);
  if (o.kind == OKV) prev = 15;
  if (prev < 0) return -1000;
  const int t = next_bar(prev >= XS ? barG(prev - XS) : barG(prev + 1));
  return t < 0 ? 5000 : t;
}
constexpr bool sched_ok() {
  int lb = -1, lc = 31, lw = -1, le = -1, ll = 11 + NS, lo = 7, ls = -1;
  for (int i = 0; i < NOPS; i++) {
    const Op& o = OPS[i];
    if (i && o.t < OPS[i - 1].t) return false;
    if (o.t <= free_t(o)) return false;
    if (o.kind == OB && o.arg < 32) { if (o.arg != ++lb) return false; }
    if (o.kind == OB && o.arg >= 32) { if (o.arg != ++lc) return false; }
    if (o.kind == OW2 && o.arg != ++lw) return false;
    if (o.kind == OAG && o.arg != ++le && le < 48) return false;
    if (o.kind == OWG && o.arg != ++ll) return false;
    if (o.kind == OWO && o.arg != ++lo) return false;
    if (o.kind == OWS && o.arg != ++ls) return false;
    if (o.kind == OKV && (o.t < POL1 || o.t > 159)) return false;
  }
  return le == 63 && ll == 63;
}
static_assert(sched_ok(), "schedule");

DI u32 slow_col(const char* p) { return (3 - ((u32)(size_t)p >> 7)) & 7; }
struct G1 {
  const char *x, *a1, *b1, *wqkv, *wg, *wu, *wd, *wo;
  int st1, e[3];
  u32 vx, v1, v2, v3, vw;
  __amdgpu_buffer_rsrc_t rw, ro; u32 we, wl, vo, sw, so, vs, sd, lb2; const char *sp, *p2;
  u32 lw, l4, r2;
  u32 ax[4], aD0, aS;
  int w;
};

template <int A> DI void issue_blk(G1& g) {
  if constexpr (A < 32) {
    constexpr int S = A / 4, R = A % 4;
    if constexpr (A == 0) glds<xslot(0)>(g.vx, g.x, g.lw);
    else if constexpr (R == 0) glds_x<xslot(S), 65536u | (128u << FLIP[S])>(g.vx, g.x, g.lw);
    else if constexpr (R == 1) glds_x<xslot(S) + 8192, 65536u>(g.vx, g.x, g.lw);
    else if constexpr (A == 2) glds<wslot(0)>(g.v1, g.a1, g.lw);
    else if constexpr (R == 2) glds_d<wslot(S), UP[S]>(g.v1, g.e[FLIP[S]] * g.st1, g.a1, g.lw);
    else glds_t<wslot(S) + 8192, 64u>(g.v1, g.b1, g.lw);
  } else if constexpr (A < 40) {
    constexpr int S = A - 32;
    if constexpr (S == 0) glds<wslot(8)>(g.v2, g.wqkv, g.lw);
    else glds_d<wslot(8 + S / 2) + (S & 1) * 8192, UP[S]>(g.v2, g.e[FLIP[S]] * 3072, g.wqkv, g.lw);
  } else {
    constexpr int S = (A - 40) >> 1;
    if constexpr (A == 40) glds<wslot(12)>(g.v3, g.wg, g.lw);
    else if constexpr (A & 1) glds_t<wslot(12 + S) + 8192, 64u>(g.v3, g.wu, g.lw);
    else glds_d<wslot(12 + S), UP[S]>(g.v3, g.e[FLIP[S]] * 3584, g.wg, g.lw);
  }
}
constexpr int kpos(int i) { return i < 12 ? i & 3 : i - 12; }
template <int K, int A> DI void issue_w2(G1& g) {
  constexpr int I = A >> 1, H = A & 1;
  const int v = g.w & 3;
  if constexpr (K == OW2) {
    const char* p = g.p2;
    const u32 lb = g.lb2;
    if constexpr (A == 0) glds<rpos(1, 0)>(g.vw, p, lb);
    else glds_x<rpos(1, 2 * I) + H * 1024, H ? 8192u : (u32)((2 * I) ^ (2 * I - 2)) << 14 | 8192u>(g.vw, p, lb);
  } else if constexpr (K == OWG) {
    if constexpr (I < 28) blds_s<rpos(1, I) + H * 1024, (I < 12 ? 64 + I : I - 12) * 16384 + H * 8192>(g.vw, g.rw, I < 12 ? g.we : g.wl, g.l4, g.sw);
    else blds_s<rpos(1, I) + H * 1024, (I - 28) * 32768 + H * 8192>(g.vo, g.ro, g.wl >> 1, g.l4, g.so);
  } else {
    blds_s<rpos(0, I) + H * 1024, I * 32768 + 16384 + H * 8192>(g.vo, g.ro, v * 131072, g.l4, g.so);
  }
}
template <int A> DI u32x4 stage_w2(G1& g) {
  const int v = g.w & 3, n = g.w >> 2;
  const char* p = g.sp;
  const u32 d = A & 1 ? 8192 : (A ? g.sd : 0);
  u32x4 r;
#define STG(c) asm volatile("v_add_u32 %1, %3, %1\n\tglobal_load_dwordx4 %0, %1, %2" : c(r), "+v"(g.vs) : "s"(p), "s"(d) : "memory")
  if constexpr (A == 0) STG("={v[112:115]}");
  else if constexpr (A == 1) STG("={v[116:119]}");
  else if constexpr (A == 2) STG("={v[120:123]}");
  else if constexpr (A == 3) STG("={v[124:127]}");
  else if constexpr (A == 4) STG("={v[240:243]}");
  else if constexpr (A == 5) STG("={v[244:247]}");
  else if constexpr (A == 6) STG("={v[248:251]}");
  else if constexpr (A == 7) STG("={v[252:255]}");
  else asm volatile("v_add_u32 %1, %3, %1\n\ts_mov_b64 exec, %4\n\tglobal_load_dwordx4 %0, %1, %2\n\ts_mov_b64 exec, -1"
                    : "=&v"(r), "+v"(g.vs) : "s"(p), "s"(d), "s"(sel<u64>(n, ~0ull, 1ull)) : "memory");
  return r;
}

struct RA { u32 a0, a1; };
template <int S> DI RA stage_addr(const G1& g) {
  RA r;
  if constexpr (S >= 8 && S < 12) { asm volatile("v_add_u32 %0, %1, %2" : "=v"(r.a0) : "i"(wslot(S)), "v"(g.aS)); r.a1 = r.a0; }
  else asm volatile("v_add_u32 %0, %2, %3\n\tv_xor_b32 %1, 32, %0" : "=&v"(r.a0), "=v"(r.a1) : "i"(wslot(S)), "v"(g.aD0));
  return r;
}
template <int S, int I> DI u32x4 read_frag(const RA& r) {
  if constexpr (S >= 8 && S < 12) return trr2(r.a0 + (16 * I) * 128, r.a0 + (16 * I + 4) * 128);
  else { u32 a = (I & 1) ? r.a1 : r.a0; return trr2(a + (16 * (I >> 1)) * 128, a + (16 * (I >> 1) + 4) * 128); }
}
template <int S, int Q> DI void read_x(G1& g, u32x4 (&xf)[32]) {
  if constexpr (S < 8) {
    if constexpr (S > 0 && !(S & 1)) asm volatile("v_xor_b32 %0, 0x10000, %0" : "+v"(g.ax[Q]));
    xf[4 * S + Q] = lds128(g.ax[Q] + (xslot(S) & 0xffffu));
  }
}

#define STAMP(i)
#define SSTAMP(i)
#define MSTAMP(i)
#define HSTAMP(i) STAMP(i)
#define TSTAMP(i)
#define LGK0 asm volatile("s_waitcnt lgkmcnt(0)" ::: "memory")
#define SBAR asm volatile("s_barrier" ::: "memory")
#define DPPADD(v, c) v += BC(float, __builtin_amdgcn_update_dpp(0, BC(int, v), c, 0xf, 0xf, true))

__global__ __launch_bounds__(512, 2) void fused_block(const char*, const char*, const char*, const char*, const char*, const char*, char*, char*) {
#ifdef __HIP_DEVICE_COMPILE__
  __shared__ __attribute__((aligned(1024))) char smem[LDS_BYTES];
  u32x16 ka_;
  const auto kptr = __builtin_amdgcn_kernarg_segment_ptr();
  asm volatile("s_load_dwordx16 %0, %1, 0x0" : "=&s"(ka_) : "s"(kptr));
  u64 pcb; asm volatile("s_getpc_b64 %0" : "=s"(pcb));
  sb();
  const u32 LB = (u32)(size_t)(LAS char*)smem;
  const int b = blockIdx.x, xcd = b & 7, idx = b >> 3;
  const int w = __builtin_amdgcn_readfirstlane(threadIdx.x >> 6);
  const u32 l = threadIdx.x & 63;
  const int m = w & 3, nh = w >> 2;
  const u32 r32 = l & 31, h = l >> 5;
  const int mi = (xcd & 1) * 8 + (idx & 7), nj = (xcd >> 1) * 4 + (idx >> 3);
  const bool isV = (nj & 3) == 3;
  const int p1 = 16 + (nj >> 2) * 3 + (nj & 3);
  const int vy = nj >> 2;
  const int m64 = xcd * 4 + (idx & 3), n64 = idx >> 2, mi2 = m64 >> 1;
  const int mia = idx & 15, hp = xcd * 2 + (idx >> 4);
  const int rot = mi & 7;

  G1 g;
  g.w = w; g.lw = LB + w * 1024; g.l4 = LB + (w & 3) * 4096;
  g.st1 = isV ? 3072 : 3584;
  const u32 row = 8 * w + (l >> 3), rowr = row + 64 * rot, p8 = l & 7, b4 = (l >> 4) & 1;
  g.vx = mi * 131072 + row * 1024 + rot * 128 + ((p8 ^ (((w & 1) << 2) | ((l >> 4) & 3))) << 4);
  g.v1 = rowr * g.st1 + (isV ? 16 + 2 * vy : p1) * 128 + ((p8 ^ (b4 << 1)) << 4);
  asm volatile("s_waitcnt lgkmcnt(0)" : "+s"(ka_), "+v"(g.vx), "+v"(g.v1), "+s"(g.lw) : "s"(kptr));
  STAMP(0); SSTAMP(0);
  g.x = ptr_of(ka_, 0); g.wqkv = ptr_of(ka_, 1); g.wo = ptr_of(ka_, 2); g.wg = ptr_of(ka_, 3); g.wu = ptr_of(ka_, 4); g.wd = ptr_of(ka_, 5);
  issue_blk<0>(g); issue_blk<1>(g);
  g.a1 = isV ? g.wqkv : g.wg;
  issue_blk<2>(g);
  g.b1 = isV ? g.wqkv + 128 : g.wu;
  issue_blk<3>(g);
  char* const out = (char*)ptr_of(ka_, 6);
  char* const ws = (char*)ptr_of(ka_, 7);
  const __amdgpu_buffer_rsrc_t rs = __builtin_amdgcn_make_buffer_rsrc(ws, 0, (int)WS_END, 0x00020000);
#pragma unroll
  for (int i = 0; i < 3; i++) g.e[i] = ((rot >> i) & 1) ? -(64 << i) : (64 << i);
#define ROT2 0
  g.r2 = ROT2 == 1 ? (n64 + 2 * (m64 & 3)) & 7 : (ROT2 == 2 ? (n64 + 5 * (m64 & 3)) & 15 : (ROT2 == 3 ? (2 * n64 + (m64 & 3)) & 15 : (ROT2 == 4 ? ((m64 & 3) * 5) ^ (2 * n64) : (ROT2 == 5 ? (m64 & 3) * 5 : (ROT2 == 6 ? ((m64 & 3) * 5) ^ n64 : 0)))));
  u32 Ev; u32x4 xr;
  const int srow = mi * 128 + nj * 8 + w;
  const u32 gq = l >> 4, rq = (l >> 2) & 3, cq = l & 3;
  const int m32 = mi * 4 + m;
  const int qt = w & 3, hd = w >> 2, head = 2 * hp + hd, TQ = mia * 4 + qt;
  const int mt = w >> 2, kq = w & 3;
  u32 abase, sx, sb0, sds, q0, q1, q2, q3;

  u32x4 xf[32], F[8];
  f32x16 acc1[2], acc2, acc3[2];
  u32x4 gat[2], gst, ag[56], oa_[8], xres, st[NS];
  u32 pol, epol;
  u32 T;
  float rr, mu, smu, cA, cB, cC, sq;
  f32x4 csa, csb;
  float c3g[4][4], c3u[4][4];
  u32x4 ob[4];
  u32 l16 = l * 16, pad, pad2, vg, va2, so, se, sl, oo, aW0, aW1, pw0, pw1, kv0, kv1, kvl;
  u32 vz0, vcs, sst, scs, scs3, cpc, cpd, ga, gb, g3, ssen, se1, sv1, de1, sqk, sfl, sgu;
  auto gsrc = [&](int r) { return r == 3 ? g3 : (r == 1 ? ga : gb + (r ? 12288 : 0)); };

  auto gu_cs = [&](u32 ub, int P) {
    const u32 a = LB + L_CS + (ub + (2 * nh + (P >> 2)) * 32 + ((P >> 1) & 1) * 8 + 4 * h) * 4;
    csa = lds128f(a); csb = lds128f(a + 64);
  };
  auto gu_store = [&](int f, int pair) {
    st128(ob[f], rs, l16, sgu + f * 1024);
  };
  float e0, e1, f0, f1, u0, u1;
  u64 emask; asm("s_cmp_eq_u32 %1, 3\n\ts_cselect_b64 %0, 0, -1" : "=s"(emask) : "s"(nj & 3) : "scc");
  auto ep_step = [&](f32x16 (&acc)[2], int P, int ph, bool first) {
    const int f = P >> 2, j = (P & 3) * 2;
    if (ph == 0) ep_a(e0, e1, acc[f][j], acc[f][j + 1], cA, cB, csa[j & 3], csa[(j + 1) & 3]);
    else if (ph == 1) {
      if (first) ob[2][P & 3] = ep_pk(e0, e1);
      ep_b(e0, e1, u0, u1, acc[f][j + 8], acc[f][j + 9], mu, csb[j & 3], csb[(j + 1) & 3]);
    } else if (ph == 2) {
      if (first) ep_cm(f0, f1, e0, e1, cC, emask); else ep_c(f0, f1, e0, e1, cC);
    } else ob[f][P & 3] = ep_fin(u0, u1, f0, f1);
  };
  auto e1_store = [&](int f, bool v) {
    const u32 lv = l16 ^ (isV ? h << 6 : 0u);
    if (!v) st128(ob[f], rs, lv, f ? se1 + de1 : se1);
    else if (!VSK) st128(ob[2], rs, lv | sel<u32>(isV, 0, OOB), sv1 + f * 65536);
    else
      asm volatile("s_cmp_eq_u32 %4, 1024\n\ts_cbranch_scc1 1f\n\tbuffer_store_dwordx4 %0, %1, %2, %3 offen sc1\n\ts_nop 1\n1:"
                   ::"v"(ob[2]), "v"(lv), "s"(rs), "s"(sv1 + f * 65536), "s"(de1) : "memory", "scc");
  };
  f32x4 cq4[4];
  auto qk_ld = [&]() {
#pragma unroll
    for (int i = 0; i < 4; i++) cq4[i] = lds128f(LB + L_CS + (128 + nh * 32 + i * 8 + 4 * h) * 4);
  };
  auto qk_step = [&](int j) {
    const f32x4& c = cq4[j >> 1];
    ob[3 * (j >> 2)][j & 3] = pk(fmaf(-mu, c[(2 * j) & 3], acc2[2 * j]) * sq, fmaf(-mu, c[(2 * j + 1) & 3], acc2[2 * j + 1]) * sq);
  };

  auto op = [&](auto I_) __attribute__((always_inline)) {
    constexpr int i = decltype(I_)::v;
    if constexpr (i >= 0) {
      constexpr int kind = OPS[i].kind, a = OPS[i].arg;
      if constexpr (kind == OB) issue_blk<a>(g);
      else if constexpr (kind == OW2 || kind == OWG || kind == OWO) issue_w2<kind, a>(g);
      else if constexpr (kind == OWS) st[a] = stage_w2<a>(g);
      else if constexpr (kind == OEV) Ev = ld1_sc1(0u, ws + WS_EP);
      else if constexpr (kind == OXR) xr = ld_asm(l16, g.x + (size_t)srow * 1024);
      else if constexpr (kind == OGAT) { if constexpr (a == 3) gst = bld4<0>(l16 & 496, rs, g3); else gat[a & 1] = bld4<a == 2 ? 12288 : 0>(vg, rs, a == 1 ? ga : gb); }
      else if constexpr (kind == OST) {
        if constexpr (a < 2) e1_store(a, false);
        else if constexpr (a < 4) st128(ob[3 * (a - 2)], rs, l16, sqk + (a - 2) * 65536);
        else if constexpr (a < 6) gu_store(a - 4, nj);
        else st128(ob[0], rs, l16, so);
      } else if constexpr (kind == OPOL) pol = bld1(pad, rs);
      else if constexpr (kind == OKV) {
        blds1<L_KV + a * 1024, a * 1024>(l16, rs, kv0, kv1, kvl);
      } else if constexpr (kind == OQ) {
        ag[a] = bld4<a * 65536>(l16, rs, WS_Q + (2 * hp * 64 + TQ) * 1024);
      } else if constexpr (kind == OAG) {
        constexpr int I = a >> 1;
        if constexpr (a == 0) se = abase + kq * 12288;
        if constexpr (I < 28) ag[a] = bld4<(((a & 1) ^ 1) * 144 + (I < 12 ? 64 + I : I - 12)) * 1024>(l16, rs, I < 12 ? se : sl);
        else oa_[a - 56] = bld4<(((a & 1) ^ 1) * 144 + 112 + 2 * (I - 28)) * 1024>(l16, rs, so);
      } else if constexpr (kind == OOA) oa_[a] = bld4<(113 + 2 * a) * 1024>(l16, rs, sl);
      else if constexpr (kind == OSV) {
        e1_store(a, true);
      } else if constexpr (kind == ORES)
        xres = ld_asm(oo, g.x + (size_t)m64 * 65536 + mt * 32768 + n64 * 128 + kq * 32);
      else if constexpr (kind == OSEN) {
        u32x4 s = {~0u, ~0u, ~0u, ~0u}; tie(s);
        st128(s, rs, l16, ssen);
      } else if constexpr (kind == OEP) { pad2 = sel<u32>(b == 0, threadIdx.x * FS + WS_F1, OOB); epol = bld1(pad2, rs); }
    }
  };
  auto ops = [&](auto T_) __attribute__((always_inline)) {
    constexpr int S = decltype(T_)::v, i = op_upto_t(S - 1), e = op_upto_t(S);
    if constexpr (S == POL1) { MSTAMP(6); wait_vm<vm_t(OSX, 4, S - 1)>(pol); MSTAMP(7); check1(pol, T, pad, rs); MSTAMP(8); }
    op(IC<(i < e ? i : -1)>{}); op(IC<(i + 1 < e ? i + 1 : -1)>{}); op(IC<(i + 2 < e ? i + 2 : -1)>{}); op(IC<(i + 3 < e ? i + 3 : -1)>{});
    op(IC<(i + 4 < e ? i + 4 : -1)>{}); op(IC<(i + 5 < e ? i + 5 : -1)>{}); op(IC<(i + 6 < e ? i + 6 : -1)>{}); op(IC<(i + 7 < e ? i + 7 : -1)>{});
  };
  auto lwait = [&](auto S_) {
    constexpr int S = decltype(S_)::v, t = barG(S);
    constexpr int n0 = vm_t(OB, S < 7 ? 4 * (S + 1) + 3 : (S == 7 ? 35 : imin(2 * S + 25, 55)), t);
    constexpr int n2 = S == CS1 / 8 - 1 ? imin(n0, vm_t(OB, 32, t)) : n0;
    if constexpr (n2 < 63) wait_vm<n2>();
  };
  u32x4 cpv;
  u32x4 cf0, cf1, ones; f32x4 cc; u32 acs, gl, f1c;
  auto linit = [&](int w) {
    const int m = w & 3, nh = w >> 2;
    *(LAS u32*)(size_t)(LB + L_ONE + threadIdx.x * 4) = (threadIdx.x & 257) ? 0u : 0x3f80u;
    const int d = (int)r32 - 4 * (int)h;
    const u32 on = nh ? 0xff800000u : 0u, off = on ^ 0xff800000u;
    lds_st128(LB + L_BD + w * 1024 + l16, u32x4{8 * m <= d ? on : off, 8 * m + 1 <= d ? on : off, 8 * m + 2 <= d ? on : off, 8 * m + 3 <= d ? on : off});
    const int k0 = (w & 1) * 2 + (w >> 1) * 8;
    *(LAS u32*)(size_t)(LB + L_MK + nh * 1024 + l16 + m * 4) = (k0 <= d ? 0xffffu : 0u) | (k0 < d ? 0xffff0000u : 0u);
  };
  auto hook = [&](auto G_) {
    constexpr int G = decltype(G_)::v;
    ops(G_);
    if constexpr (G == PFT) {
      u32 t, c;
      asm volatile("s_lshl_b32 %1, %2, 8\n\ts_add_u32 %1, %1, %3\n\ts_and_b32 %1, %1, 0x7ff\n\ts_cmp_lt_u32 %1, 288\n\ts_cbranch_scc0 1f\n\t"
                   "s_cmp_lt_u32 %1, 224\n\ts_cselect_b64 vcc, %5, %6\n\ts_cselect_b32 m0, 0, 224\n\ts_sub_u32 %1, %1, m0\n\ts_lshl_b32 %1, %1, 13\n\ts_mov_b32 m0, %7\n\t"
                   "v_lshl_add_u32 %0, %4, 3, %1\n\tglobal_load_lds_dword %0, vcc\n1:"
                   : "=&v"(t), "=&s"(c) : "s"(w), "s"(b + (8 - PFW) * 256), "v"(l16), "s"(g.wd), "s"(g.wo), "i"(L_PF) : "memory", "scc", "vcc");
    }
    if constexpr (G == PFT + 2) {
      u32 t, c;
      asm volatile("s_cmp_eq_u32 %2, %9\n\ts_cbranch_scc0 1f\n\ts_bitcmp0_b32 %3, 0\n\ts_cselect_b64 vcc, %5, %6\n\tv_mul_u32_u24 %0, 224, %4\n\ts_mul_i32 %1, %8, 1792\n\t"
                   "s_add_u32 %1, %1, %10\n\ts_lshl_b32 %1, %1, 7\n\ts_mov_b32 m0, %7\n\tv_add_u32 %0, %1, %0\n\tglobal_load_lds_dword %0, vcc\n1:"
                   : "=&v"(t), "=&s"(c) : "s"(w), "s"(b), "v"(l16), "s"(g.wg), "s"(g.wu), "i"(L_PF), "s"(rot), "i"(PFW + 2), "s"(nj) : "memory", "scc", "vcc");
    }
    if constexpr (C3T > 0 && G == C3T) {
      u32 t, c;
      asm volatile("s_cmp_eq_u32 %2, %8\n\ts_cbranch_scc0 1f\n\ts_lshr_b32 %1, %3, 3\n\ts_sub_u32 %1, %1, 12\n\ts_cmp_lt_u32 %1, 12\n\ts_cbranch_scc1 1f\n\t"
                   "s_cmp_lt_u32 %3, 96\n\ts_cselect_b64 vcc, %5, %6\n\ts_cselect_b32 m0, 28, -12\n\ts_add_u32 %1, %1, m0\n\ts_lshl_b32 %1, %1, 16\n\t"
                   "s_sub_u32 m0, 0x1ff, vcc_lo\n\ts_and_b32 m0, m0, 0x380\n\ts_add_u32 %1, %1, m0\n\ts_mov_b32 m0, %7\n\t"
                   "v_lshl_add_u32 %0, %4, 6, %1\n\tglobal_load_lds_dword %0, vcc\n1:"
                   : "=&v"(t), "=&s"(c) : "s"(w), "s"(b), "v"(l16), "s"(g.wd), "s"(g.wo), "i"(L_PF), "i"(C3W) : "memory", "scc", "vcc");
    }
    if constexpr (CPL > 0 && G == CPL) {
      u32 t, c;
      asm volatile("s_cmp_eq_u32 %3, %8\n\ts_cbranch_scc0 1f\n\ts_cmp_lt_u32 %5, 224\n\ts_cselect_b64 vcc, %6, %7\n\ts_cselect_b32 m0, 0, 224\n\tv_and_b32 %1, 0x380, %4\n\ts_sub_u32 m0, %5, m0\n\t"
                   "s_sub_u32 %2, 0x1ff, vcc_lo\n\tv_mad_u32_u24 %1, %1, 7, %4\n\ts_lshl_b32 m0, m0, 13\n\ts_and_b32 %2, %2, 0x380\n\ts_add_u32 %2, %2, m0\n\tv_add_u32 %1, %2, %1\n\tglobal_load_dwordx4 %0, %1, vcc\n1:"
                   : "=&v"(cpv), "=&v"(t), "=&s"(c) : "s"(cpc), "v"(l16), "s"(cpd), "s"(g.wd), "s"(g.wo), "i"(CPB) : "memory", "scc", "vcc");
    }
    if constexpr (CPL > 0 && G == CPS) {
      u32 t;
      asm volatile("s_cmp_eq_u32 %2, %7\n\ts_cbranch_scc0 1f\n\tv_add_u32 %0, %5, %3\n\ts_waitcnt vmcnt(%6)\n\tglobal_store_dwordx4 %0, %1, %4 sc1\n\ts_nop 1\n1:"
                   : "=&v"(t) : "v"(cpv), "s"(cpc), "v"(l16), "s"(ws), "s"(WS_CP + (cpd << (13 - CPD))), "i"(cnt_range(op_upto_t(CPL), op_upto_t(CPS - 1), 0)), "i"(CPB) : "memory", "scc");
    }
    constexpr int SG = G - 2;
    if constexpr (SG == 0) {
      wait_vm<vm_h(OXR, 0, G)>(Ev, xr);
      T = __builtin_amdgcn_readfirstlane(Ev) + 0x9E3779B9u;
      smu = bflo(xr.x) + bfhi(xr.x);
    }
    if constexpr (SG >= 1 && SG < 4) { smu += bflo(xr[SG]) + bfhi(xr[SG]); tie(smu); }
    if constexpr (SG == 4) { DPPADD(smu, 0xB1); DPPADD(smu, 0x4E); }
    if constexpr (SG == 5) { DPPADD(smu, 0x141); DPPADD(smu, 0x140); }
    if constexpr (SG == 6) { u32x2 r = __builtin_amdgcn_permlane16_swap(asu(smu), asu(smu), false, false); smu = asf(r.x) + asf(r.y); }
    if constexpr (SG == 7) smu = xsum32(smu) * (1.f / 512.f);
    if constexpr (SG >= 8 && SG < 16) {
      const u32 v = xr[(SG - 8) >> 1];
      const float d = ((SG & 1) ? bfhi(v) : bflo(v)) - smu;
      if constexpr (SG == 8) sq = d * d; else sq = fmaf(d, d, sq);
    }
    if constexpr (SG == 19) { DPPADD(sq, 0xB1); DPPADD(sq, 0x4E); }
    if constexpr (SG == 20) { DPPADD(sq, 0x141); DPPADD(sq, 0x140); }
    if constexpr (SG == 21) { u32x2 r = __builtin_amdgcn_permlane16_swap(asu(sq), asu(sq), false, false); sq = asf(r.x) + asf(r.y); }
    if constexpr (SG == 22) sq = __builtin_amdgcn_rsqf(fmaf(xsum32(sq), 1.f / 512.f, 1e-5f));
    if constexpr (G == op_time(OSX, 0)) {
      u32x4 o = {asu(smu), T, asu(sq), T}; st128(o, rs, vz0, sst);
    }
    {
      constexpr int R = G >= CS3 - 4, C = G - (R ? CS3 : CS1);
      if constexpr (R && C == -2) { acs = LB + wslot(12) + nh * 8192 + (4 * gq + rq) * 128 + ((m ^ ((rq >> 1) | (nh << 1))) << 5) + cq * 8; tie(acs); }
      if constexpr (R && C == -1) { ones = u32x4{0x3f803f80u, 0x3f803f80u, 0x3f803f80u, 0x3f803f80u}; tie(ones); }
      if constexpr (C == 0) cf0 = trr2(acs, acs + 2048);
      if constexpr (C == 2) cf1 = trr2(acs + 4096, acs + 6144);
      if constexpr (C == 3) { const f32x4 z = {}; sb(); cc = mfma16(ones, cf0, z); sb(); }
      if constexpr (C == 5) { sb(); cc = mfma16(ones, cf1, cc); sb(); }
      if constexpr (G == op_time(OSX, 1 + R)) {
        u32x2 o = {asu(cc[0]), T};
        __builtin_amdgcn_raw_buffer_store_b64(o, rs, R ? sel<u32>(l < 16 && mi < 8, l * 8, OOB) : vcs, R ? scs3 : scs, 16);
      }
    }
    if constexpr (G == op_time(OGAT, 0) - 1 || G == op_time(OGAT, 2) - 1) { vg = p8 * 512 + (l >> 3) * 16 + m * 128; tie(vg); }
    if constexpr (G == GA || G == GC || G == G3) {
      constexpr int R = G == GA ? 0 : (G == GC ? 1 : 2);
      wait_vm<vm_h(OGAT, R, G)>(gat[R & 1]);
      check4(gat[R & 1], T, vg + gsrc(R), ws, rs);
      gl = LB + L_CS + (R == 1 ? 128 + m * 16 : (R ? 192 : 0) + m * 32 + nh * 16) * 4 + (l >> 3) * 8;
    }
    if constexpr (G == GA + 1 || G == GC + 1 || G == G3 + 1) {
      u32x4& q = gat[G == GC + 1];
      u32 a = q.x, c = q.z;
      asm volatile("v_add_f32_dpp %0, %0, %0 quad_perm:[1,0,3,2] row_mask:0xf bank_mask:0xf\n\tv_add_f32_dpp %1, %1, %1 quad_perm:[1,0,3,2] row_mask:0xf bank_mask:0xf\n\ts_nop 0\n\t"
                   "v_add_f32_dpp %0, %0, %0 quad_perm:[2,3,0,1] row_mask:0xf bank_mask:0xf\n\tv_add_f32_dpp %1, %1, %1 quad_perm:[2,3,0,1] row_mask:0xf bank_mask:0xf"
                   : "+v"(a), "+v"(c));
      q.x = a; q.z = c;
    }
    if constexpr (G == GA + 2 || G == GC + 2 || G == G3 + 2) {
      u32x4& q = gat[G == GC + 2];
      u32 a = q.x, c;
      asm volatile("v_add_f32_dpp %0, %0, %0 row_half_mirror row_mask:0xf bank_mask:0xf\n\tv_add_f32_dpp %1, %2, %2 row_half_mirror row_mask:0xf bank_mask:0xf"
                   : "+v"(a), "=v"(c) : "v"(q.z));
      if constexpr (WM) asm volatile("s_mov_b32 exec_lo, 0x1010101\n\ts_mov_b32 exec_hi, 0x1010101\n\tds_write_b64 %0, %1\n\ts_mov_b64 exec, -1" ::"v"(gl), "v"(u32x2{a, c}) : "memory");
      else *(LAS u32x2*)(size_t)gl = u32x2{a, c};
    }
    if constexpr (G == GS) {
      wait_vm<vm_h(OGAT, 3, G)>(gst);
      check4(gst, T, (l16 & 496) + gsrc(3), ws, rs);
      mu = asf(gst.x); rr = asf(gst.z);
    }
    if constexpr (G == EP1 - 1) { cA = (isV ? 1.f : -LOG2E) * rr; cB = -cA * mu; cC = __builtin_amdgcn_rcpf(rr); f0 = f1 = rr; gu_cs(0, 0); }
    if constexpr (MV < 4 && G > EP1 && G <= EP1 + 25) {
      constexpr int P = (G - EP1 - 1) / 3, ph = (G - EP1 - 1) % 3;
      if constexpr (P < 8) ep_step(acc1, P, ph, true);
      if constexpr (ph == 0 && P > 0) ep_step(acc1, P - 1, 3, true);
      if constexpr (ph == 2 && (P & 1) && P < 6) gu_cs(0, P + 1);
    }
    if constexpr (MV >= 4 && G > EP1 && G <= EP1 + 32) {
      constexpr int P = (G - EP1 - 1) / 4, ph = (G - EP1 - 1) % 4;
      ep_step(acc1, P, ph, true);
      if constexpr (ph == 2 && (P & 1) && P < 6) gu_cs(0, P + 1);
    }
    if constexpr (G == barG(9) + 1 || G == barG(13)) __builtin_amdgcn_s_setprio(1);
    if constexpr (G == QKE + 4 || G == F1G + 3) __builtin_amdgcn_s_setprio(0);
    if constexpr (G == 95) { qk_ld(); sq = rr * (nj < 8 ? 0.25f * LOG2E : 1.f); }
    if constexpr (G >= QKE && G < QKE + 4) { qk_step(2 * (G - QKE)); qk_step(2 * (G - QKE) + 1); }
    if constexpr (G == QKE + 4) { MSTAMP(1); }
    if constexpr (G == F1G + 3) { MSTAMP(4); }
    if constexpr (G == POL0) { MSTAMP(5); }
    if constexpr (G == F1G) { MSTAMP(2); wait_vm<vm_h(OST, 3, G)>(); MSTAMP(3); f1c = cnt_add(LB + L_CNT + 12, 1u); }
    if constexpr (G == F1G + 2) {
      constexpr int RD[8] = {2, 4, 2, 0, 2, 2, 2, 2};
      asm volatile("s_waitcnt lgkmcnt(%1)" : "+v"(f1c) : "i"(RD[G & 7] + RD[(G - 1) & 7]) : "memory");
      __builtin_amdgcn_raw_buffer_store_b32(T, rs, sel<u32>(f1c == 7, 0, OOB), sfl, 16);
    }
    if constexpr (G == 103) { g.vs = (l >> 3) * 1024 + n64 * 128 + ((p8 ^ (b4 << 2)) << 4); tie(g.vs); }
    if constexpr (G == 120) { g.vw = (g.vs & 8191) | nh << 14; tie(g.vw); }
    auto op_ = [](int v) { ties(v); return v; };
    if constexpr (G == 9) { const int n = op_(nj); ga = WS_CS + (n * 5 + 2) * 4096; gb = WS_CS + (n * 5 + nh) * 4096; ties(ga); ties(gb); }
    if constexpr (G == 13) { scs3 = op_(rot) * 512 + m * 128; scs = sel(op_(mi) < 8, gb, ga) + scs3; ties(scs); ties(scs3); }
    if constexpr (G == 15) { const int y = op_(vy), q = rot * 8 + w; cpd = sel(q < 48, 32 * y - 16, 16 * y + 176); ties(cpd); }
    if constexpr (G == 17) { const int y = op_(vy), q = rot * 8 + w; cpd = q + sel<u32>(q < 16, 136 + 24 * y, cpd); cpc = (mi & 8) | (nj & 3); ties(cpd); ties(cpc); }
    if constexpr (G == 18) { sst = WS_ST + op_(srow) * 16; vz0 = sel<u32>(l == 0, 0, OOB); ties(sst); tie(vz0); }
    if constexpr (G == 25) { const int i_ = op_(mi); vcs = sel<u32>(l < sel<u32>(i_ & w * 2 & 8, 0, 16), l * 8, OOB); tie(vcs); }
    if constexpr (G == 102) { const u32 n = op_(n64) * 128; g.sw = CPL && (((u32)(size_t)g.wd + n) & 896) == 384 ? CPD : 0; g.so = CPL && (((u32)(size_t)g.wo + n) & 896) == 384 ? CPD : 0; ties(g.sw); ties(g.so); }
    if constexpr (G == 104) { const int p = op_(hp); q0 = w < 4 ? 8 + (p >> 1) : (p >> 2) * 4 + 3; q1 = op_(mia) + (w & 1); ties(q0); ties(q1); }
    if constexpr (G == 105) { q0 = (max(q1, 2u) * 16 - 32 + q0) | (max(q1, 1u) * 16 - 16 + q0) << 8; ties(q0); }
    if constexpr (G == 106) { q1 = op_(mi2) * 16 + 4 * kq; q2 = op_(mia) * 16 + (hp >> 1); q3 = sel<u32>((g.sw | g.so) && w >= 4, 13 * 16, 5 * 16); ties(q1); ties(q2); ties(q3); }
    if constexpr (G == 123) { pad = sel<u32>(l16 < 80, q1 - 2 + (l16 >> 4), l16 + ((CPB & 8) * 16 + (CPB & 3) - 80) + 4 * kq); tie(pad); }
    if constexpr (G == 126) { pad = sel<u32>(l16 < 32, (q0 >> ((l16 >> 1) & 8)) & 255, sel<u32>(w < 4, q2, pad)); tie(pad); }
    if constexpr (G == POL0 - 1) { pad = sel<u32>(l16 < q3, pad * FS + WS_F1, OOB); tie(pad); }
    if constexpr (G == LIG) linit(op_(w));
    if constexpr (G == 35) { g3 = WS_ST + op_(m32) * 512; ties(g3); }
    if constexpr (G == 30) { scs3 += gb + 12288; ties(scs3); }
    if constexpr (G == 31) { ssen = WS_ACT + (op_(TQ) * 144 + 112 + head) * 1024; ties(ssen); }
    if constexpr (G == 33) { const int n = op_(nh), k = op_(kq); sx = sel(n, k * 196608 + 70 * 16384, k * 131072 + 16384); g.sd = sel(n, 8192, 24576); ties(sx); ties(g.sd); }
    if constexpr (G == 34) { g.sp = sel(op_(nh), g.wd, g.wo) + sx; ties(g.sp); }
    if constexpr (G == 37) { g.p2 = g.wd + (64 + 12 * op_(kq)) * 16384; ties(g.p2); }
    if constexpr (G == 39) { const int v = op_(w); g.lb2 = g.l4 + (v >> 2) * 2048; kvl = LB + (((v >> 1) & 1) * 4 + (v >> 2) * 2 + (v & 1)) * 6144; ties(g.lb2); ties(kvl); }
    if constexpr (G == 41) { const int v = op_(w); kv0 = WS_K + (v >> 2) * (2u << 20) + (2 * hp + ((v >> 1) & 1)) * 65536; ties(kv0); }
    if constexpr (G == 47) { kv1 = kv0 + (mia * 4 - 8 + (op_(w) & 1) * 6) * 1024; ties(kv1); }
    if constexpr (G == 49) { sv1 = WS_V + (u32)(((2 * op_(vy) * 4 + 2 * nh) * 64) + m32) * 1024; ties(sv1); }
    if constexpr (G == 55) { sgu = WS_ACT + (op_(m32) * 144 + 2 * nh) * 1024; ties(sgu); }
    if constexpr (G == 57) { const bool v = (op_(nj) & 3) == 3; se1 = sel<u32>(v, sv1 + 4 * 65536, sgu + p1 * 4096); de1 = v ? 65536 : 1024; ties(se1); ties(de1); }
    if constexpr (G == 61) { sqk = WS_Q + (u32)((op_(nj) * 4 + nh * 2) * 64 + m32) * 1024; ties(sqk); }
    if constexpr (G == 93) { const int n = op_(nj); sfl = WS_F1 + (mi * 16 + n) * FS; sgu += n * 4096; ties(sfl); ties(sgu); }
    if constexpr (G == 94) { const int n = op_(nh), k = op_(kq); sb0 = sel<u32>(n, g.l4, LB + (k & 1) * 8192); sx = n * k << 5; ties(sb0); ties(sx); }
    if constexpr (G == 96) { abase = WS_ACT + (u32)(op_(m64) * 2) * 144 * 1024; sds = (3 + 2 * op_(nh)) * SLOT; ties(abase); ties(sds); }
    if constexpr (G == POL0) pol = bld1(pad, rs);
    if constexpr (G == E3R - 1) { gl = LB + L_CS + (192 + 2 * nh * 32 + 4 * h) * 4; tie(gl); cA = -LOG2E * rr; cB = -cA * mu; }
    if constexpr (G >= E3R && G < E3R + 4) {
      constexpr int P = G - E3R;
      const f32x4 a = lds128f(gl + (P >> 1) * 128 + (P & 1) * 32), c = lds128f(gl + (P >> 1) * 128 + (P & 1) * 32 + 64);
#pragma unroll
      for (int i = 0; i < 4; i++) { c3g[P][i] = a[i]; c3u[P][i] = c[i]; }
    }
    if constexpr (G == 143) {
      aW0 = sb0 + (l16 & 528) + ((l16 & 192) << 1) + ((l16 >> 2) & 8); tie(aW0);
    }
    if constexpr (G == 145) { aW0 += (((l16 >> 1) & 64) | ((l16 >> 3) & 32)) ^ sx; aW1 = aW0 + sds; tie(aW0); tie(aW1); }
    if constexpr (G == 147) { pw0 = g.l4 + l16; pw1 = pw0 + sds; oo = r32 * 1024 + h * 16; tie(pw0); tie(pw1); tie(oo); }
    if constexpr (G == 149) {
      const u32 a = g.vs & 8191, c = (l16 & 896) | (a & 127);
      g.vw = sel(g.sw != 0, c, a); g.vo = sel(g.so != 0, c, a); tie(g.vw); tie(g.vo);
    }
    if constexpr (G == 138) { q0 = WS_F1 + ((q1 & 127) + (q1 >> 7)) * FS; q1 = WS_F2 + q1 * FS; ties(q0); ties(q1); }
    if constexpr (G == 151) { pad = ((l16 & 32) << 9) + ((l16 & 16) << 4) + q0; tie(pad); }
    if constexpr (G == 153) { pad = sel<u32>(l16 < 64, q1 + l16 * 8, sel<u32>(l16 < 128, pad, OOB)); tie(pad); }
    if constexpr (G >= E3M && G < E3M + 8) ew_m(c3g[(G - E3M) >> 1][(G - E3M & 1) * 2], c3g[(G - E3M) >> 1][(G - E3M & 1) * 2 + 1], cB);
  };
  auto mm = [&](auto S_, auto I_) {
    constexpr int S = decltype(S_)::v, I = decltype(I_)::v;
    const f32x16 z = {};
    sb();
    if constexpr (S >= 8 && S < 12) acc2 = mfma(F[I], xf[8 * (S - 8) + I], (S == 8 && I == 0) ? z : acc2);
    else if constexpr (S < 8) acc1[I & 1] = mfma(F[I], xf[4 * S + (I >> 1)], (S == 0 && I < 2) ? z : acc1[I & 1]);
    else acc3[I & 1] = mfma(F[I], xf[4 * (S - 12) + (I >> 1)], (S == 12 && I < 2) ? z : acc3[I & 1]);
    sb();
  };
  RA ra;
  auto gap = [&](auto S_, auto I_) {
    constexpr int S = decltype(S_)::v, I = decltype(I_)::v, N = S < 19 ? S + 1 : 0;
    mm(S_, I_);
    if constexpr (I == 0) F[4] = read_frag<S, 4>(ra);
    else if constexpr (I == 1) { F[5] = read_frag<S, 5>(ra); F[6] = read_frag<S, 6>(ra); }
    else if constexpr (I == 2) F[7] = read_frag<S, 7>(ra);
    else if constexpr (I >= 4 && S < 19) {
      read_x<N, I - 4>(g, xf);
      if constexpr (S < 7) sb();
      F[I - 4] = read_frag<N, I - 4>(ra);
    }
    hook(IC<8 * S + I>{});
    if constexpr (I == 3) {
      if constexpr (S < 19) ra = stage_addr<N>(g);
      if constexpr (has_bar(S)) { lwait(S_); SBAR; }
      SSTAMP(S + 2);
    }
  };
  auto stage = [&](auto S_) {
    gap(S_, IC<0>{}); gap(S_, IC<1>{}); gap(S_, IC<2>{}); gap(S_, IC<3>{}); gap(S_, IC<4>{}); gap(S_, IC<5>{}); gap(S_, IC<6>{}); gap(S_, IC<7>{});
  };
  HSTAMP(1);
  {
    g.v2 = rowr * 3072 + nj * 128 + ((p8 ^ (b4 << 2)) << 4);
    g.v3 = rowr * 3584 + nj * 128 + ((p8 ^ (b4 << 1)) << 4);
    const u32 gb = gq & 1, xrow = 32 * m + r32, f = (xrow >> 1) & 7;
#pragma unroll
    for (int q = 0; q < 4; q++) { g.ax[q] = LB + xrow * 128 + (((2 * q + h) ^ f) << 4); tie(g.ax[q]); }
    g.aD0 = LB + gb * 8192 + (8 * h + rq) * 128 + (((2 * nh) ^ ((rq >> 1) | (gb << 1))) << 5) + cq * 8;
    g.aS = LB + (8 * h + rq) * 128 + ((nh ^ (rq >> 1)) << 6) + gb * 32 + cq * 8;
    acs = LB + sel<u32>(mi < 8, wslot(CS1 / 8) + nh * 8192, wslot(8)) + (4 * gq + rq) * 128 + ((m ^ sel<u32>(mi < 8, (rq >> 1) | (nh << 1), rq & 2)) << 5) + cq * 8;
    ones = u32x4{0x3f803f80u, 0x3f803f80u, 0x3f803f80u, 0x3f803f80u};
    tie(g.v2); tie(g.v3); tie(g.aD0); tie(g.aS); tie(acs); tie(ones);
    if constexpr (LIG < 0) linit(w);
  }
  ra = stage_addr<0>(g);
  sb();
  ops(IC<-100>{});
  { u32 t; asm volatile("s_cmp_lt_u32 %1, %5\n\ts_cbranch_scc0 1f\n\ts_lshl_b32 vcc_lo, %1, 10\n\ts_and_b32 vcc_lo, vcc_lo, 0xe000\n\ts_mov_b32 m0, %4\n\tv_lshl_add_u32 %0, %2, 3, vcc_lo\n\tglobal_load_lds_dword %0, %3\n1:"
                   : "=&v"(t) : "s"(((w ^ 1) << 8) + b), "v"(l16), "s"(pcb), "i"(L_PF), "i"(CPF * 8) : "memory", "scc", "vcc"); }
  wait_vm<vm_t(OB, 3, -100)>(); bar();
  HSTAMP(2); SSTAMP(1);
  read_x<0, 0>(g, xf); F[0] = read_frag<0, 0>(ra); F[1] = read_frag<0, 1>(ra); read_x<0, 1>(g, xf);
  F[2] = read_frag<0, 2>(ra); F[3] = read_frag<0, 3>(ra); read_x<0, 2>(g, xf); read_x<0, 3>(g, xf);
  stage(IC<0>{}); stage(IC<1>{}); stage(IC<2>{}); HSTAMP(3); stage(IC<3>{}); stage(IC<4>{}); stage(IC<5>{}); HSTAMP(4);
  stage(IC<6>{}); stage(IC<7>{}); HSTAMP(5); stage(IC<8>{}); stage(IC<9>{}); stage(IC<10>{}); stage(IC<11>{});
  HSTAMP(6);
  stage(IC<12>{}); stage(IC<13>{}); stage(IC<14>{}); stage(IC<15>{});
  stage(IC<16>{}); stage(IC<17>{}); stage(IC<18>{}); stage(IC<19>{});
  SSTAMP(22);
  HSTAMP(7); TSTAMP(1); MSTAMP(9);
  f32x16 bd, bf;
  u32 pr[5];
  auto unstage = [&](int k, int r, int i) {
    const u32 p = rpos(r, i) + (k & 1) * 1024;
    lds_st128(p >= 4 * SLOT ? pw1 + (p - (3 + 2 * r) * SLOT) : pw0 + p, st[k]);
  };
  wait_vm<vm_t(OWS, NS - 1, 159)>();
  asm volatile("s_nop 4");
  u32 bdb = LB + L_BD + l16;
  auto ep3 = [&](auto B_) {
    constexpr int B = decltype(B_)::v;
    auto piece = [&](auto P_) {
      constexpr int P = decltype(P_)::v, f = P >> 1, j = (P & 1) * 4;
      const f32x16& a = acc3[f];
      ew_a(c3g[P], a[j], a[j + 1], a[j + 2], a[j + 3], cA);
      ops(IC<B + 8 * f + 2 * (P & 1)>{});
      ew_b(c3g[P], c3u[P], a[j + 8], a[j + 9], a[j + 10], a[j + 11], mu);
      ops(IC<B + 8 * f + 2 * (P & 1) + 1>{});
      if constexpr (B == PA(0)) {
        const f32x4 t0 = lds128f(bdb + P * 1024), t1 = lds128f(bdb + (L_BF - L_BD) + P * 1024);
#pragma unroll
        for (int i = 0; i < 4; i++) { bd[4 * P + i] = t0[i]; bf[4 * P + i] = t1[i]; }
      }
      const u32x2 o = ew_c(c3g[P], c3u[P], cC);
      ob[f][j / 2] = o.x; ob[f][j / 2 + 1] = o.y;
    };
    piece(IC<0>{}); piece(IC<1>{}); piece(IC<2>{}); piece(IC<3>{});
    ops(IC<B + 16>{});
  };
  f32x16 c2[2][2];
  const f32x16 zero = {};
  auto w2frag = [&](int r, int i, int nt) {
    const u32 p = r ? rpos(1, i) : rpos(0, i & 3) + (i >> 2) * 4096, a = ((p >= 4 * SLOT ? aW1 : aW0) ^ (nt * 64u)) + (p >= 4 * SLOT ? p - (3 + 2 * r) * SLOT : p);
    return trr2(a, a + 1024);
  };
  auto publish = [&]() {
    u32 old = cnt_add(LB + L_CNT, 1u);
    asm volatile("s_waitcnt lgkmcnt(0)" : "+v"(old)::"memory");
    __builtin_amdgcn_raw_buffer_store_b32(T, rs, sel<u32>(old == 7, 0, OOB), sfl + (WS_F2 - WS_F1), 16);
  };
  auto fin_addr = [&](int r) {
    u32 x = l16; tie(x);
    pw0 = g.l4 + x; pw1 = pw0 + (3 + 2 * r) * SLOT;
#pragma unroll
    for (int d = r; d < 4; d++) { pr[d + 1] = LB + rpos(1, d + 3 - 4 * r) + (kq ^ d) * 4096 + x; tie(pr[d + 1]); }
    pr[0] = LB + rpos(0, 4) + (kq >> 1) * SLOT + (kq & 1) * 2048 + r * 8192 + x; tie(pr[0]);
    oo = ((x & 496) << 6) + (x >> 9) * 16;
    asm volatile("" : "+v"(pw0), "+v"(pw1), "+v"(oo));
  };
  auto put = [&](int r, int n) {
    const f32x16& d = c2[n >> 3][(n >> 2) & 1];
    const int q = n & 3;
    const u32 p = rpos(r, (n >> 1) + 4 - 5 * r) + (n & 1) * 1024;
    lds_st128(p >= 4 * SLOT ? pw1 + (p - (3 + 2 * r) * SLOT) : pw0 + p, u32x4{asu(d[4 * q]), asu(d[4 * q + 1]), asu(d[4 * q + 2]), asu(d[4 * q + 3])});
  };
  auto fin = [&](auto R_) __attribute__((always_inline)) {
  constexpr int r = decltype(R_)::v;
  STAMP(13);
  {
    u32 ep = sel<u32>(b == 0 && w == 0 && oo == 0, WS_EP, OOB), tv = T;
    char* ob_ = out + (size_t)m64 * 65536 + mt * 32768 + n64 * 128 + kq * 32;
    asm volatile("" : "+v"(ep), "+v"(tv), "+s"(ob_));
    bar();
    STAMP(14);
    f32x4 p0[6], p1[6];
#pragma unroll
    for (int d = r; d < 6; d++) { const u32 a = d < 4 ? pr[d + 1] : pr[0] + (d - 4) * 4096; p0[d] = lds128f(a); p1[d] = lds128f(a + 1024); }
    __builtin_amdgcn_raw_buffer_store_b32(tv, rs, ep, 0, 16);
    sb();
    const f32x16& c = c2[r ^ 1][0];
    f32x4 lo = f32x4{c[0], c[1], c[2], c[3]}, hi = f32x4{c[4], c[5], c[6], c[7]};
#pragma unroll
    for (int d = r; d < 6; d++) { lo += p0[d]; hi += p1[d]; }
    u32x4 o = {pk(lo[0], lo[1]), pk(lo[2], lo[3]), pk(hi[0], hi[1]), pk(hi[2], hi[3])};
    asm volatile("global_store_dwordx4 %0, %1, %2" ::"v"(oo), "v"(o), "s"(ob_) : "memory");
  }
  };
  auto lds_spin = [&](u32 a, u32 n) {
#pragma unroll 1
    for (int it = 0; it < SPIN; it++) {
      if (__builtin_amdgcn_readfirstlane(*(volatile LAS u32*)(size_t)a) == n) break;
      __builtin_amdgcn_s_sleep(1);
    }
  };
  auto ocheck = [&](auto R_) __attribute__((always_inline)) {
    constexpr int r = decltype(R_)::v, K = r ? OAG : OOA, A = r ? 56 : 0, P = r ? PG(0) : PA(0);
    if (__builtin_expect(unset(oa_), OE & 1)) {
#pragma unroll 1
      for (int it = 0; it < SPIN; it++) {
        op(IC<op_find(K, A, P)>{}); op(IC<op_find(K, A + 1, P)>{}); op(IC<op_find(K, A + 2, P)>{}); op(IC<op_find(K, A + 3, P)>{});
        op(IC<op_find(K, A + 4, P)>{}); op(IC<op_find(K, A + 5, P)>{}); op(IC<op_find(K, A + 6, P)>{}); op(IC<op_find(K, A + 7, P)>{});
        wait8(oa_);
        if (!unset(oa_)) break;
      }
    }
  };
  if (w < 4) {
    if constexpr (EAPRIO) __builtin_amdgcn_s_setprio(EAPRIO);
    tie(bdb);
    ep3(IC<PA(0)>{});
    u32x16 ad0; u32x4 ad1;
    u32x4 mk0 = lds128(bdb + (L_MK - L_BD)), mk1 = lds128(bdb + (L_MK - L_BD) + 1024);
    {
      const bool vl = r32 < 16;
      const u32 vst = vl ? 1024u : 0u;
      const u32 va = sel<u32>(vl, LB + L_KV + 12288 + qt * 1024 + (l & 1) * 512 + (((4 * h + rq) ^ (4 * (l & 1))) << 4) + ((l >> 1) & 1) * 8, LB + L_ONE + 128 + (l & 15) * 8);
#pragma unroll
      for (int i = 0; i < 18; i++) {
        u32 a;
        asm("v_mad_u32_u24 %0, %1, %2, %3" : "=v"(a) : "v"(vst), "i"(i % 9 + i / 9 * 24), "v"(va));
        if (i < 16) ad0[i] = a; else ad1[i - 16] = a;
      }
      ad1[2] = LB + L_KV + qt * 1024 + l16; ad1[3] = bdb;
    }
    so = WS_ACT + (TQ * 144 + 112 + 2 * hp) * 1024;
    const u32 sf = sfl + (WS_F2 - WS_F1);
    u32 cn = LB + L_CNT, one = 1, tv = T;
    asm volatile("" : "+{v[80:95]}"(bd), "+{v[96:111]}"(bf), "+{v[208:223]}"(ad0), "+{v[224:227]}"(ad1), "+{v[228:231]}"(mk0), "+{v[232:235]}"(mk1), "+v"(cn), "+v"(one), "+v"(tv), "+v"(pw0), "+v"(l16), "+v"(pw1), "+v"(aW0), "+v"(aW1),
                 "+{v[112:115]}"(st[0]), "+{v[116:119]}"(st[1]), "+{v[120:123]}"(st[2]), "+{v[124:127]}"(st[3]));
    if (__builtin_expect(TQ < 8, TQE)) {
      const u32 vz = LB + L_ONE + 1024 + (l16 >> 1 & 255);
#pragma unroll
      for (int i = 0; i < 18; i++) {
        const bool ok = TQ - 8 + i % 9 >= 0;
        if (i < 16) ad0[i] = sel(ok, ad0[i], vz); else ad1[i - 16] = sel(ok, ad1[i - 16], vz);
      }
      asm volatile("" : "+{v[80:95]}"(bd), "+{v[96:111]}"(bf), "+{v[208:223]}"(ad0), "+{v[224:227]}"(ad1), "+{v[228:231]}"(mk0), "+{v[232:235]}"(mk1), "+v"(cn), "+v"(one), "+v"(tv), "+v"(pw0), "+v"(l16), "+v"(pw1), "+v"(aW0), "+v"(aW1),
                 "+{v[112:115]}"(st[0]), "+{v[116:119]}"(st[1]), "+{v[120:123]}"(st[2]), "+{v[124:127]}"(st[3]));
    }
    if constexpr (APF) {
      const int j = idx - 24;
      glds_pf(l16 * 8, sel(j < 0, g.wd + (32 * qt + 8 + idx) * 8192, g.wo + (16 * qt + 4 * (j >> 1) + (j & 1)) * 8192), LB + L_PF, ~0ull);
      if constexpr (APF > 1) glds_pf(l16 * 8, g.wd + (32 * qt + (idx & 7)) * 8192, LB + L_PF, sel<u64>(idx < 8, ~0ull, 1ull));
    }
    ops(IC<PA(2, 1)>{});
    MSTAMP(10);
    wait_vm<vm_t(OQ, 1, PA(2, 1)) + APF>(ag[0], ag[1]);
    MSTAMP(11);
    SBAR;
    MSTAMP(12);
    __builtin_amdgcn_s_setprio(APRIO);
    HSTAMP(8); TSTAMP(2); SSTAMP(23);
    asm volatile(
      "ds_read_b128 v[176:179], v226 offset:8192\nds_read_b128 v[144:147], v226\nds_read_b128 v[148:151], v226 offset:1024\nds_read_b128 v[152:155], v226 offset:2048\n"
      "ds_read_b128 v[156:159], v226 offset:3072\nds_read_b128 v[160:163], v226 offset:4096\nds_read_b128 v[164:167], v226 offset:5120\nds_read_b128 v[168:171], v226 offset:6144\n"
      "ds_read_b128 v[172:175], v226 offset:7168\ns_waitcnt lgkmcnt(8)\nv_mfma_f32_32x32x16_bf16 v[80:95], v[176:179], v[180:183], v[80:95]\ns_waitcnt lgkmcnt(7)\n"
      "v_mfma_f32_32x32x16_bf16 v[96:111], v[144:147], v[180:183], v[96:111]\ns_waitcnt lgkmcnt(6)\nv_mfma_f32_32x32x16_bf16 v[48:63], v[148:151], v[180:183], 0\n"
      "s_waitcnt lgkmcnt(5)\nv_mfma_f32_32x32x16_bf16 v[64:79], v[152:155], v[180:183], 0\nds_write_b128 %[pw], v[112:115] offset:16384\nds_write_b128 %[pw], v[116:119] offset:17408\n"
      "ds_write_b128 %[pw], v[120:123] offset:18432\nds_write_b128 %[pw], v[124:127] offset:19456\ns_nop 3\nv_max_f32_e32 v80, v80, v96\nv_max_f32_e32 v81, v81, v97\n"
      "v_max_f32_e32 v82, v82, v98\nv_max_f32_e32 v83, v83, v99\nv_max_f32_e32 v84, v84, v100\nv_max_f32_e32 v85, v85, v101\nv_max_f32_e32 v86, v86, v102\nv_max_f32_e32 v87, v87, v103\n"
      "v_max_f32_e32 v88, v88, v104\nv_max_f32_e32 v89, v89, v105\nv_max_f32_e32 v90, v90, v106\nv_max_f32_e32 v91, v91, v107\nv_max_f32_e32 v92, v92, v108\nv_max_f32_e32 v93, v93, v109\n"
      "v_max_f32_e32 v94, v94, v110\nv_max_f32_e32 v95, v95, v111\ns_waitcnt lgkmcnt(0)\nv_mfma_f32_32x32x16_bf16 v[96:111], v[156:159], v[180:183], 0\nv_max3_f32 v236, v48, v49, v50\n"
      "v_max3_f32 v237, v51, v52, v53\nv_max3_f32 v236, v236, v54, v55\nv_max3_f32 v237, v237, v56, v57\nv_max3_f32 v236, v236, v58, v59\nv_max3_f32 v237, v237, v60, v61\n"
      "v_max3_f32 v236, v236, v62, v63\nv_mfma_f32_32x32x16_bf16 v[48:63], v[160:163], v[180:183], 0\nv_max3_f32 v236, v236, v64, v65\nv_max3_f32 v237, v237, v66, v67\n"
      "v_max3_f32 v236, v236, v68, v69\nv_max3_f32 v237, v237, v70, v71\nv_max3_f32 v236, v236, v72, v73\nv_max3_f32 v237, v237, v74, v75\nv_max3_f32 v236, v236, v76, v77\n"
      "v_max3_f32 v237, v237, v78, v79\nv_mfma_f32_32x32x16_bf16 v[64:79], v[164:167], v[180:183], 0\nv_max3_f32 v236, v236, v96, v97\nv_max3_f32 v237, v237, v98, v99\n"
      "v_max3_f32 v236, v236, v100, v101\nv_max3_f32 v237, v237, v102, v103\nv_max3_f32 v236, v236, v104, v105\nv_max3_f32 v237, v237, v106, v107\nv_max3_f32 v236, v236, v108, v109\n"
      "v_max3_f32 v237, v237, v110, v111\nv_mfma_f32_32x32x16_bf16 v[96:111], v[168:171], v[180:183], 0\nv_max3_f32 v236, v236, v48, v49\nv_max3_f32 v237, v237, v50, v51\n"
      "v_max3_f32 v236, v236, v52, v53\nv_max3_f32 v237, v237, v54, v55\nv_max3_f32 v236, v236, v56, v57\nv_max3_f32 v237, v237, v58, v59\nv_max3_f32 v236, v236, v60, v61\n"
      "v_max3_f32 v237, v237, v62, v63\nv_mfma_f32_32x32x16_bf16 v[48:63], v[172:175], v[180:183], 0\nv_max3_f32 v236, v236, v80, v81\nv_max3_f32 v237, v237, v82, v83\n"
      "v_max3_f32 v236, v236, v84, v85\nv_max3_f32 v237, v237, v86, v87\nv_max3_f32 v236, v236, v88, v89\nv_max3_f32 v237, v237, v90, v91\nv_max3_f32 v236, v236, v92, v93\n"
      "v_max3_f32 v237, v237, v94, v95\nv_max3_f32 v236, v236, v64, v65\nv_max3_f32 v237, v237, v66, v67\nv_max3_f32 v236, v236, v68, v69\nv_max3_f32 v237, v237, v70, v71\n"
      "v_max3_f32 v236, v236, v72, v73\nv_max3_f32 v237, v237, v74, v75\nv_max3_f32 v236, v236, v76, v77\nv_max3_f32 v237, v237, v78, v79\nv_max3_f32 v236, v236, v96, v97\n"
      "v_max3_f32 v237, v237, v98, v99\nv_max3_f32 v236, v236, v100, v101\nv_max3_f32 v237, v237, v102, v103\nv_max3_f32 v236, v236, v104, v105\nv_max3_f32 v237, v237, v106, v107\n"
      "v_max3_f32 v236, v236, v108, v109\nv_max3_f32 v237, v237, v110, v111\nv_max3_f32 v236, v236, v48, v49\nv_max3_f32 v237, v237, v50, v51\nv_max3_f32 v236, v236, v52, v53\n"
      "v_max3_f32 v237, v237, v54, v55\nv_max3_f32 v236, v236, v56, v57\nv_max3_f32 v237, v237, v58, v59\nv_max3_f32 v236, v236, v60, v61\nv_max3_f32 v237, v237, v62, v63\n"
      "s_waitcnt vmcnt(%[np])\ns_mov_b64 exec, 1\nds_add_rtn_u32 v239, %[cn], %[one]\ns_mov_b64 exec, -1\nv_max_f32_e32 v236, v236, v237\nds_read_b64_tr_b16 v[188:189], v208\n"
      "v_mov_b32_e32 v237, v236\nds_read_b64_tr_b16 v[190:191], v208 offset:128\nds_read_b64_tr_b16 v[104:105], v216\nv_permlane32_swap_b32_e32 v236, v237\nds_read_b64_tr_b16 v[106:107], v216 offset:128\n"
      "v_max_f32_e32 v238, v236, v237\nds_read_b64_tr_b16 v[196:197], v208 offset:256\nv_xor_b32_e32 v128, 0x80000000, v238\nv_xor_b32_e32 v129, 0x80000000, v238\n"
      "ds_read_b64_tr_b16 v[198:199], v208 offset:384\nv_mov_b64_e32 v[130:131], v[128:129]\nv_mov_b64_e32 v[132:133], v[128:129]\nv_mov_b64_e32 v[134:135], v[128:129]\n"
      "v_mov_b64_e32 v[136:137], v[128:129]\nv_mov_b64_e32 v[138:139], v[128:129]\nv_mov_b64_e32 v[140:141], v[128:129]\nv_mov_b64_e32 v[142:143], v[128:129]\n"
      "v_sub_f32_e32 v80, v80, v238\nv_sub_f32_e32 v81, v81, v238\nv_mfma_f32_32x32x16_bf16 v[64:79], v[148:151], v[180:183], v[128:143]\nv_sub_f32_e32 v82, v82, v238\n"
      "v_sub_f32_e32 v83, v83, v238\ns_waitcnt lgkmcnt(0)\nv_readfirstlane_b32 vcc_lo, v239\ns_cmp_eq_u32 vcc_lo, 7\ns_cbranch_scc0 1f\ns_mov_b64 exec, 1\nbuffer_store_dword %[tv], off, %[rs], %[sf] sc1\n"
      "s_mov_b64 exec, -1\n1:\nv_exp_f32_e64 v80, v80 clamp\nds_read_b64_tr_b16 v[108:109], v216 offset:256\nv_exp_f32_e64 v81, v81 clamp\nds_read_b64_tr_b16 v[110:111], v216 offset:384\n"
      "v_exp_f32_e64 v82, v82 clamp\nv_sub_f32_e32 v84, v84, v238\nv_exp_f32_e64 v83, v83 clamp\nv_sub_f32_e32 v85, v85, v238\nv_cvt_pk_bf16_f32 v200, v80, v81\n"
      "v_sub_f32_e32 v86, v86, v238\nv_cvt_pk_bf16_f32 v201, v82, v83\nv_sub_f32_e32 v87, v87, v238\nv_exp_f32_e64 v84, v84 clamp\nds_read_b64_tr_b16 v[192:193], v209\n"
      "v_exp_f32_e64 v85, v85 clamp\nds_read_b64_tr_b16 v[194:195], v209 offset:128\nv_exp_f32_e64 v86, v86 clamp\nv_and_b32_e32 v96, v228, v200\nv_exp_f32_e64 v87, v87 clamp\n"
      "v_and_b32_e32 v97, v229, v201\nv_cvt_pk_bf16_f32 v202, v84, v85\nv_xor_b32_e32 v200, v96, v200\nv_cvt_pk_bf16_f32 v203, v86, v87\nv_xor_b32_e32 v201, v97, v201\n"
      "v_and_b32_e32 v98, v230, v202\nv_and_b32_e32 v99, v231, v203\nv_xor_b32_e32 v202, v98, v202\nv_xor_b32_e32 v203, v99, v203\nv_sub_f32_e32 v88, v88, v238\n"
      "v_sub_f32_e32 v89, v89, v238\nv_mfma_f32_32x32x16_bf16 v[112:127], v[188:191], v[200:203], 0\nv_sub_f32_e32 v90, v90, v238\nv_sub_f32_e32 v91, v91, v238\n"
      "v_exp_f32_e64 v88, v88 clamp\nv_sub_f32_e32 v92, v92, v238\nv_exp_f32_e64 v89, v89 clamp\nv_sub_f32_e32 v93, v93, v238\nv_exp_f32_e64 v90, v90 clamp\nv_sub_f32_e32 v94, v94, v238\n"
      "v_exp_f32_e64 v91, v91 clamp\nv_sub_f32_e32 v95, v95, v238\nv_mfma_f32_32x32x16_bf16 v[112:127], v[104:107], v[96:99], v[112:127]\nv_cvt_pk_bf16_f32 v204, v88, v89\n"
      "v_cvt_pk_bf16_f32 v205, v90, v91\nv_exp_f32_e64 v92, v92 clamp\nv_and_b32_e32 v100, v232, v204\nv_exp_f32_e64 v93, v93 clamp\nv_and_b32_e32 v101, v233, v205\n"
      "v_exp_f32_e64 v94, v94 clamp\nv_xor_b32_e32 v204, v100, v204\nv_exp_f32_e64 v95, v95 clamp\nv_xor_b32_e32 v205, v101, v205\nv_cvt_pk_bf16_f32 v206, v92, v93\n"
      "v_cvt_pk_bf16_f32 v207, v94, v95\nv_and_b32_e32 v102, v234, v206\nv_and_b32_e32 v103, v235, v207\nv_xor_b32_e32 v206, v102, v206\nv_xor_b32_e32 v207, v103, v207\n"
      "v_exp_f32_e64 v64, v64 clamp\nv_exp_f32_e64 v65, v65 clamp\nv_mfma_f32_32x32x16_bf16 v[112:127], v[196:199], v[204:207], v[112:127]\nv_exp_f32_e64 v66, v66 clamp\n"
      "v_exp_f32_e64 v67, v67 clamp\nv_cvt_pk_bf16_f32 v200, v64, v65\nv_exp_f32_e64 v68, v68 clamp\nv_mfma_f32_32x32x16_bf16 v[48:63], v[152:155], v[180:183], v[128:143]\n"
      "v_exp_f32_e64 v69, v69 clamp\nv_cvt_pk_bf16_f32 v201, v66, v67\nv_exp_f32_e64 v70, v70 clamp\nv_exp_f32_e64 v71, v71 clamp\ns_waitcnt lgkmcnt(0)\nv_mfma_f32_32x32x16_bf16 v[112:127], v[108:111], v[100:103], v[112:127]\n"
      "v_cvt_pk_bf16_f32 v202, v68, v69\nv_exp_f32_e64 v72, v72 clamp\nds_read_b64_tr_b16 v[196:197], v209 offset:256\nv_cvt_pk_bf16_f32 v203, v70, v71\nv_exp_f32_e64 v73, v73 clamp\n"
      "ds_read_b64_tr_b16 v[198:199], v209 offset:384\nv_exp_f32_e64 v74, v74 clamp\nds_read_b64_tr_b16 v[188:189], v210\nv_mfma_f32_32x32x16_bf16 v[112:127], v[192:195], v[200:203], v[112:127]\n"
      "v_exp_f32_e64 v75, v75 clamp\nds_read_b64_tr_b16 v[190:191], v210 offset:128\nv_cvt_pk_bf16_f32 v204, v72, v73\nv_exp_f32_e64 v76, v76 clamp\nds_read_b128 v[148:151], v226 offset:25600\n"
      "v_exp_f32_e64 v77, v77 clamp\nds_read_b128 v[176:179], v226 offset:32768\nv_cvt_pk_bf16_f32 v205, v74, v75\nv_exp_f32_e64 v78, v78 clamp\nv_exp_f32_e64 v79, v79 clamp\n"
      "v_cvt_pk_bf16_f32 v206, v76, v77\nv_exp_f32_e64 v48, v48 clamp\nv_exp_f32_e64 v49, v49 clamp\nv_cvt_pk_bf16_f32 v207, v78, v79\nv_mfma_f32_32x32x16_bf16 v[64:79], v[156:159], v[180:183], v[128:143]\n"
      "v_exp_f32_e64 v50, v50 clamp\nv_exp_f32_e64 v51, v51 clamp\nv_cvt_pk_bf16_f32 v200, v48, v49\nv_exp_f32_e64 v52, v52 clamp\nv_exp_f32_e64 v53, v53 clamp\n"
      "s_waitcnt lgkmcnt(0)\nv_mfma_f32_32x32x16_bf16 v[112:127], v[196:199], v[204:207], v[112:127]\nv_cvt_pk_bf16_f32 v201, v50, v51\nv_exp_f32_e64 v54, v54 clamp\n"
      "ds_read_b64_tr_b16 v[196:197], v210 offset:256\nv_exp_f32_e64 v55, v55 clamp\nds_read_b64_tr_b16 v[198:199], v210 offset:384\nv_cvt_pk_bf16_f32 v202, v52, v53\n"
      "v_exp_f32_e64 v56, v56 clamp\nds_read_b64_tr_b16 v[192:193], v211\nv_cvt_pk_bf16_f32 v203, v54, v55\nv_exp_f32_e64 v57, v57 clamp\nds_read_b64_tr_b16 v[194:195], v211 offset:128\n"
      "v_exp_f32_e64 v58, v58 clamp\nds_read_b128 v[152:155], v226 offset:26624\nv_mfma_f32_32x32x16_bf16 v[112:127], v[188:191], v[200:203], v[112:127]\nv_exp_f32_e64 v59, v59 clamp\n"
      "ds_read_b128 v[144:147], v226 offset:24576\nv_cvt_pk_bf16_f32 v204, v56, v57\nv_exp_f32_e64 v60, v60 clamp\nv_exp_f32_e64 v61, v61 clamp\nv_cvt_pk_bf16_f32 v205, v58, v59\n"
      "v_exp_f32_e64 v62, v62 clamp\nv_exp_f32_e64 v63, v63 clamp\nv_cvt_pk_bf16_f32 v206, v60, v61\nv_exp_f32_e64 v64, v64 clamp\nv_exp_f32_e64 v65, v65 clamp\n"
      "v_cvt_pk_bf16_f32 v207, v62, v63\nv_mfma_f32_32x32x16_bf16 v[48:63], v[160:163], v[180:183], v[128:143]\nv_exp_f32_e64 v66, v66 clamp\nv_exp_f32_e64 v67, v67 clamp\n"
      "v_cvt_pk_bf16_f32 v200, v64, v65\nv_exp_f32_e64 v68, v68 clamp\nv_exp_f32_e64 v69, v69 clamp\ns_waitcnt lgkmcnt(0)\nv_mfma_f32_32x32x16_bf16 v[112:127], v[196:199], v[204:207], v[112:127]\n"
      "v_cvt_pk_bf16_f32 v201, v66, v67\nv_exp_f32_e64 v70, v70 clamp\nds_read_b64_tr_b16 v[196:197], v211 offset:256\nv_exp_f32_e64 v71, v71 clamp\nds_read_b64_tr_b16 v[198:199], v211 offset:384\n"
      "v_cvt_pk_bf16_f32 v202, v68, v69\nv_exp_f32_e64 v72, v72 clamp\nds_read_b64_tr_b16 v[188:189], v212\nv_cvt_pk_bf16_f32 v203, v70, v71\nv_exp_f32_e64 v73, v73 clamp\n"
      "ds_read_b64_tr_b16 v[190:191], v212 offset:128\nv_exp_f32_e64 v74, v74 clamp\nds_read_b128 v[156:159], v226 offset:27648\nv_mfma_f32_32x32x16_bf16 v[112:127], v[192:195], v[200:203], v[112:127]\n"
      "v_exp_f32_e64 v75, v75 clamp\nds_read_b128 v[80:83], v227\nv_cvt_pk_bf16_f32 v204, v72, v73\nv_exp_f32_e64 v76, v76 clamp\nds_read_b128 v[84:87], v227 offset:1024\n"
      "v_exp_f32_e64 v77, v77 clamp\nv_cvt_pk_bf16_f32 v205, v74, v75\nv_exp_f32_e64 v78, v78 clamp\nv_exp_f32_e64 v79, v79 clamp\nv_cvt_pk_bf16_f32 v206, v76, v77\n"
      "v_exp_f32_e64 v48, v48 clamp\nv_exp_f32_e64 v49, v49 clamp\nv_cvt_pk_bf16_f32 v207, v78, v79\nv_mfma_f32_32x32x16_bf16 v[64:79], v[164:167], v[180:183], v[128:143]\n"
      "v_exp_f32_e64 v50, v50 clamp\nv_exp_f32_e64 v51, v51 clamp\nv_cvt_pk_bf16_f32 v200, v48, v49\nv_exp_f32_e64 v52, v52 clamp\nv_exp_f32_e64 v53, v53 clamp\n"
      "s_waitcnt lgkmcnt(0)\nv_mfma_f32_32x32x16_bf16 v[112:127], v[196:199], v[204:207], v[112:127]\nv_cvt_pk_bf16_f32 v201, v50, v51\nv_exp_f32_e64 v54, v54 clamp\n"
      "ds_read_b64_tr_b16 v[196:197], v212 offset:256\nv_exp_f32_e64 v55, v55 clamp\nds_read_b64_tr_b16 v[198:199], v212 offset:384\nv_cvt_pk_bf16_f32 v202, v52, v53\n"
      "v_exp_f32_e64 v56, v56 clamp\nds_read_b64_tr_b16 v[192:193], v213\nv_cvt_pk_bf16_f32 v203, v54, v55\nv_exp_f32_e64 v57, v57 clamp\nds_read_b64_tr_b16 v[194:195], v213 offset:128\n"
      "v_exp_f32_e64 v58, v58 clamp\nds_read_b128 v[160:163], v226 offset:28672\nv_mfma_f32_32x32x16_bf16 v[112:127], v[188:191], v[200:203], v[112:127]\nv_exp_f32_e64 v59, v59 clamp\n"
      "ds_read_b128 v[88:91], v227 offset:2048\nv_cvt_pk_bf16_f32 v204, v56, v57\nv_exp_f32_e64 v60, v60 clamp\nds_read_b128 v[92:95], v227 offset:3072\nv_exp_f32_e64 v61, v61 clamp\n"
      "v_cvt_pk_bf16_f32 v205, v58, v59\nv_exp_f32_e64 v62, v62 clamp\nv_exp_f32_e64 v63, v63 clamp\nv_cvt_pk_bf16_f32 v206, v60, v61\nv_exp_f32_e64 v64, v64 clamp\n"
      "v_exp_f32_e64 v65, v65 clamp\nv_cvt_pk_bf16_f32 v207, v62, v63\nv_mfma_f32_32x32x16_bf16 v[48:63], v[168:171], v[180:183], v[128:143]\nv_exp_f32_e64 v66, v66 clamp\n"
      "v_exp_f32_e64 v67, v67 clamp\nv_cvt_pk_bf16_f32 v200, v64, v65\nv_exp_f32_e64 v68, v68 clamp\nv_exp_f32_e64 v69, v69 clamp\ns_waitcnt lgkmcnt(0)\nv_mfma_f32_32x32x16_bf16 v[112:127], v[196:199], v[204:207], v[112:127]\n"
      "v_cvt_pk_bf16_f32 v201, v66, v67\nv_exp_f32_e64 v70, v70 clamp\nds_read_b64_tr_b16 v[196:197], v213 offset:256\nv_exp_f32_e64 v71, v71 clamp\nds_read_b64_tr_b16 v[198:199], v213 offset:384\n"
      "v_cvt_pk_bf16_f32 v202, v68, v69\nv_exp_f32_e64 v72, v72 clamp\nds_read_b64_tr_b16 v[188:189], v214\nv_cvt_pk_bf16_f32 v203, v70, v71\nv_exp_f32_e64 v73, v73 clamp\n"
      "ds_read_b64_tr_b16 v[190:191], v214 offset:128\nv_exp_f32_e64 v74, v74 clamp\nds_read_b128 v[164:167], v226 offset:29696\nv_mfma_f32_32x32x16_bf16 v[112:127], v[192:195], v[200:203], v[112:127]\n"
      "v_exp_f32_e64 v75, v75 clamp\nds_read_b128 v[96:99], v227 offset:4096\nv_cvt_pk_bf16_f32 v204, v72, v73\nv_exp_f32_e64 v76, v76 clamp\nds_read_b128 v[100:103], v227 offset:5120\n"
      "v_exp_f32_e64 v77, v77 clamp\nv_cvt_pk_bf16_f32 v205, v74, v75\nv_exp_f32_e64 v78, v78 clamp\nv_exp_f32_e64 v79, v79 clamp\nv_cvt_pk_bf16_f32 v206, v76, v77\n"
      "v_exp_f32_e64 v48, v48 clamp\nv_exp_f32_e64 v49, v49 clamp\nv_cvt_pk_bf16_f32 v207, v78, v79\nv_mfma_f32_32x32x16_bf16 v[64:79], v[172:175], v[180:183], v[128:143]\n"
      "v_exp_f32_e64 v50, v50 clamp\nv_exp_f32_e64 v51, v51 clamp\nv_cvt_pk_bf16_f32 v200, v48, v49\nv_exp_f32_e64 v52, v52 clamp\nv_exp_f32_e64 v53, v53 clamp\n"
      "s_waitcnt lgkmcnt(0)\nv_mfma_f32_32x32x16_bf16 v[112:127], v[196:199], v[204:207], v[112:127]\nv_cvt_pk_bf16_f32 v201, v50, v51\nv_exp_f32_e64 v54, v54 clamp\n"
      "ds_read_b64_tr_b16 v[196:197], v214 offset:256\nv_exp_f32_e64 v55, v55 clamp\nds_read_b64_tr_b16 v[198:199], v214 offset:384\nv_cvt_pk_bf16_f32 v202, v52, v53\n"
      "v_exp_f32_e64 v56, v56 clamp\nds_read_b64_tr_b16 v[192:193], v215\nv_cvt_pk_bf16_f32 v203, v54, v55\nv_exp_f32_e64 v57, v57 clamp\nds_read_b64_tr_b16 v[194:195], v215 offset:128\n"
      "v_exp_f32_e64 v58, v58 clamp\nds_read_b128 v[168:171], v226 offset:30720\nv_mfma_f32_32x32x16_bf16 v[112:127], v[188:191], v[200:203], v[112:127]\nv_exp_f32_e64 v59, v59 clamp\n"
      "ds_read_b128 v[104:107], v227 offset:6144\nv_cvt_pk_bf16_f32 v204, v56, v57\nv_exp_f32_e64 v60, v60 clamp\nds_read_b128 v[108:111], v227 offset:7168\nv_exp_f32_e64 v61, v61 clamp\n"
      "v_cvt_pk_bf16_f32 v205, v58, v59\nv_exp_f32_e64 v62, v62 clamp\nv_exp_f32_e64 v63, v63 clamp\nv_cvt_pk_bf16_f32 v206, v60, v61\nv_exp_f32_e64 v64, v64 clamp\n"
      "v_exp_f32_e64 v65, v65 clamp\nv_cvt_pk_bf16_f32 v207, v62, v63\nv_exp_f32_e64 v66, v66 clamp\nv_mfma_f32_32x32x16_bf16 v[80:95], v[176:179], v[184:187], v[80:95]\n"
      "v_exp_f32_e64 v67, v67 clamp\nv_cvt_pk_bf16_f32 v200, v64, v65\nv_exp_f32_e64 v68, v68 clamp\nv_exp_f32_e64 v69, v69 clamp\ns_waitcnt lgkmcnt(0)\nv_mfma_f32_32x32x16_bf16 v[112:127], v[196:199], v[204:207], v[112:127]\n"
      "v_cvt_pk_bf16_f32 v201, v66, v67\nv_exp_f32_e64 v70, v70 clamp\nds_read_b64_tr_b16 v[196:197], v215 offset:256\nv_exp_f32_e64 v71, v71 clamp\nds_read_b64_tr_b16 v[198:199], v215 offset:384\n"
      "v_mfma_f32_32x32x16_bf16 v[96:111], v[144:147], v[184:187], v[96:111]\nv_cvt_pk_bf16_f32 v202, v68, v69\nv_exp_f32_e64 v72, v72 clamp\nds_read_b128 v[172:175], v226 offset:31744\n"
      "v_cvt_pk_bf16_f32 v203, v70, v71\nv_exp_f32_e64 v73, v73 clamp\nv_exp_f32_e64 v74, v74 clamp\nv_mfma_f32_32x32x16_bf16 v[112:127], v[192:195], v[200:203], v[112:127]\n"
      "v_exp_f32_e64 v75, v75 clamp\nv_cvt_pk_bf16_f32 v204, v72, v73\nv_exp_f32_e64 v76, v76 clamp\nv_exp_f32_e64 v77, v77 clamp\nv_mfma_f32_32x32x16_bf16 v[48:63], v[148:151], v[184:187], 0\n"
      "v_cvt_pk_bf16_f32 v205, v74, v75\nv_exp_f32_e64 v78, v78 clamp\nv_exp_f32_e64 v79, v79 clamp\nv_cvt_pk_bf16_f32 v206, v76, v77\nv_cvt_pk_bf16_f32 v207, v78, v79\n"
      "v_max_f32_e32 v80, v80, v96\nv_max_f32_e32 v81, v81, v97\ns_waitcnt lgkmcnt(0)\nv_mfma_f32_32x32x16_bf16 v[112:127], v[196:199], v[204:207], v[112:127]\n"
      "v_max_f32_e32 v82, v82, v98\nv_max_f32_e32 v83, v83, v99\nv_max_f32_e32 v84, v84, v100\nv_max_f32_e32 v85, v85, v101\nv_max_f32_e32 v86, v86, v102\nv_max_f32_e32 v87, v87, v103\n"
      "v_mfma_f32_32x32x16_bf16 v[64:79], v[152:155], v[184:187], 0\nv_max_f32_e32 v88, v88, v104\nv_max_f32_e32 v89, v89, v105\nv_max_f32_e32 v90, v90, v106\n"
      "v_max_f32_e32 v91, v91, v107\nv_max_f32_e32 v92, v92, v108\nv_max_f32_e32 v93, v93, v109\nv_max_f32_e32 v94, v94, v110\nv_max_f32_e32 v95, v95, v111\nv_mfma_f32_32x32x16_bf16 v[96:111], v[156:159], v[184:187], 0\n"
      "v_rcp_f32_e32 v239, v120\ns_nop 0\nv_mul_f32_e32 v112, v112, v239\nv_mul_f32_e32 v113, v113, v239\nv_mul_f32_e32 v114, v114, v239\nv_mul_f32_e32 v115, v115, v239\n"
      "v_mul_f32_e32 v116, v116, v239\nv_mul_f32_e32 v117, v117, v239\nv_mul_f32_e32 v118, v118, v239\nv_mul_f32_e32 v119, v119, v239\nv_cvt_pk_bf16_f32 v200, v112, v113\n"
      "v_cvt_pk_bf16_f32 v201, v114, v115\nv_cvt_pk_bf16_f32 v202, v116, v117\nv_cvt_pk_bf16_f32 v203, v118, v119\nv_min_u32_e32 v200, -2, v200\nv_min_u32_e32 v201, -2, v201\n"
      "v_min_u32_e32 v202, -2, v202\nv_min_u32_e32 v203, -2, v203\nbuffer_store_dwordx4 v[200:203], %[l16], %[rs], %[so] offen sc1\ns_mov_b64 exec, 1\nds_add_u32 %[cn], %[one] offset:8\n"
      "s_mov_b64 exec, -1\nv_max3_f32 v236, v48, v49, v50\nv_max3_f32 v237, v51, v52, v53\nv_max3_f32 v236, v236, v54, v55\nv_max3_f32 v237, v237, v56, v57\nv_max3_f32 v236, v236, v58, v59\n"
      "v_max3_f32 v237, v237, v60, v61\nv_max3_f32 v236, v236, v62, v63\nv_mfma_f32_32x32x16_bf16 v[48:63], v[160:163], v[184:187], 0\nv_max3_f32 v236, v236, v64, v65\n"
      "v_max3_f32 v237, v237, v66, v67\nv_max3_f32 v236, v236, v68, v69\nv_max3_f32 v237, v237, v70, v71\nv_max3_f32 v236, v236, v72, v73\nv_max3_f32 v237, v237, v74, v75\n"
      "v_max3_f32 v236, v236, v76, v77\nv_max3_f32 v237, v237, v78, v79\nv_mfma_f32_32x32x16_bf16 v[64:79], v[164:167], v[184:187], 0\nv_max3_f32 v236, v236, v96, v97\n"
      "v_max3_f32 v237, v237, v98, v99\nv_max3_f32 v236, v236, v100, v101\nv_max3_f32 v237, v237, v102, v103\nv_max3_f32 v236, v236, v104, v105\nv_max3_f32 v237, v237, v106, v107\n"
      "v_max3_f32 v236, v236, v108, v109\nv_max3_f32 v237, v237, v110, v111\nv_mfma_f32_32x32x16_bf16 v[96:111], v[168:171], v[184:187], 0\nv_max3_f32 v236, v236, v48, v49\n"
      "v_max3_f32 v237, v237, v50, v51\nv_max3_f32 v236, v236, v52, v53\nv_max3_f32 v237, v237, v54, v55\nv_max3_f32 v236, v236, v56, v57\nv_max3_f32 v237, v237, v58, v59\n"
      "v_max3_f32 v236, v236, v60, v61\nv_max3_f32 v237, v237, v62, v63\nv_mfma_f32_32x32x16_bf16 v[48:63], v[172:175], v[184:187], 0\nv_max3_f32 v236, v236, v80, v81\n"
      "v_max3_f32 v237, v237, v82, v83\nv_max3_f32 v236, v236, v84, v85\nv_max3_f32 v237, v237, v86, v87\nv_max3_f32 v236, v236, v88, v89\nv_max3_f32 v237, v237, v90, v91\n"
      "v_max3_f32 v236, v236, v92, v93\nv_max3_f32 v237, v237, v94, v95\nv_max3_f32 v236, v236, v64, v65\nv_max3_f32 v237, v237, v66, v67\nv_max3_f32 v236, v236, v68, v69\n"
      "v_max3_f32 v237, v237, v70, v71\nv_max3_f32 v236, v236, v72, v73\nv_max3_f32 v237, v237, v74, v75\nv_max3_f32 v236, v236, v76, v77\nv_max3_f32 v237, v237, v78, v79\n"
      "v_max3_f32 v236, v236, v96, v97\nv_max3_f32 v237, v237, v98, v99\nv_max3_f32 v236, v236, v100, v101\nv_max3_f32 v237, v237, v102, v103\nv_max3_f32 v236, v236, v104, v105\n"
      "v_max3_f32 v237, v237, v106, v107\nv_max3_f32 v236, v236, v108, v109\nv_max3_f32 v237, v237, v110, v111\nv_max3_f32 v236, v236, v48, v49\nv_max3_f32 v237, v237, v50, v51\n"
      "v_max3_f32 v236, v236, v52, v53\nv_max3_f32 v237, v237, v54, v55\nv_max3_f32 v236, v236, v56, v57\nv_max3_f32 v237, v237, v58, v59\nv_max3_f32 v236, v236, v60, v61\n"
      "v_max3_f32 v237, v237, v62, v63\nv_max_f32_e32 v236, v236, v237\nds_read_b64_tr_b16 v[188:189], v217\nv_mov_b32_e32 v237, v236\nds_read_b64_tr_b16 v[190:191], v217 offset:128\n"
      "ds_read_b64_tr_b16 v[104:105], v225\nv_permlane32_swap_b32_e32 v236, v237\nds_read_b64_tr_b16 v[106:107], v225 offset:128\nv_max_f32_e32 v238, v236, v237\n"
      "ds_read_b64_tr_b16 v[196:197], v217 offset:256\nv_xor_b32_e32 v128, 0x80000000, v238\nv_xor_b32_e32 v129, 0x80000000, v238\nds_read_b64_tr_b16 v[198:199], v217 offset:384\n"
      "v_mov_b64_e32 v[130:131], v[128:129]\nv_mov_b64_e32 v[132:133], v[128:129]\nv_mov_b64_e32 v[134:135], v[128:129]\nv_mov_b64_e32 v[136:137], v[128:129]\n"
      "v_mov_b64_e32 v[138:139], v[128:129]\nv_mov_b64_e32 v[140:141], v[128:129]\nv_mov_b64_e32 v[142:143], v[128:129]\nv_sub_f32_e32 v80, v80, v238\nv_sub_f32_e32 v81, v81, v238\n"
      "v_mfma_f32_32x32x16_bf16 v[64:79], v[148:151], v[184:187], v[128:143]\nv_sub_f32_e32 v82, v82, v238\nv_sub_f32_e32 v83, v83, v238\nv_exp_f32_e64 v80, v80 clamp\n"
      "ds_read_b64_tr_b16 v[108:109], v225 offset:256\nv_exp_f32_e64 v81, v81 clamp\nds_read_b64_tr_b16 v[110:111], v225 offset:384\nv_exp_f32_e64 v82, v82 clamp\n"
      "v_sub_f32_e32 v84, v84, v238\nv_exp_f32_e64 v83, v83 clamp\nv_sub_f32_e32 v85, v85, v238\nv_cvt_pk_bf16_f32 v200, v80, v81\nv_sub_f32_e32 v86, v86, v238\n"
      "v_cvt_pk_bf16_f32 v201, v82, v83\nv_sub_f32_e32 v87, v87, v238\nv_exp_f32_e64 v84, v84 clamp\nds_read_b64_tr_b16 v[192:193], v218\nv_exp_f32_e64 v85, v85 clamp\n"
      "ds_read_b64_tr_b16 v[194:195], v218 offset:128\nv_exp_f32_e64 v86, v86 clamp\nv_and_b32_e32 v96, v228, v200\nv_exp_f32_e64 v87, v87 clamp\nv_and_b32_e32 v97, v229, v201\n"
      "v_cvt_pk_bf16_f32 v202, v84, v85\nv_xor_b32_e32 v200, v96, v200\nv_cvt_pk_bf16_f32 v203, v86, v87\nv_xor_b32_e32 v201, v97, v201\nv_and_b32_e32 v98, v230, v202\n"
      "v_and_b32_e32 v99, v231, v203\nv_xor_b32_e32 v202, v98, v202\nv_xor_b32_e32 v203, v99, v203\nv_sub_f32_e32 v88, v88, v238\nv_sub_f32_e32 v89, v89, v238\n"
      "s_waitcnt lgkmcnt(0)\nv_mfma_f32_32x32x16_bf16 v[112:127], v[188:191], v[200:203], 0\nv_sub_f32_e32 v90, v90, v238\nv_sub_f32_e32 v91, v91, v238\nv_exp_f32_e64 v88, v88 clamp\n"
      "v_sub_f32_e32 v92, v92, v238\nv_exp_f32_e64 v89, v89 clamp\nv_sub_f32_e32 v93, v93, v238\nv_exp_f32_e64 v90, v90 clamp\nv_sub_f32_e32 v94, v94, v238\nv_exp_f32_e64 v91, v91 clamp\n"
      "v_sub_f32_e32 v95, v95, v238\nv_mfma_f32_32x32x16_bf16 v[112:127], v[104:107], v[96:99], v[112:127]\nv_cvt_pk_bf16_f32 v204, v88, v89\nv_cvt_pk_bf16_f32 v205, v90, v91\n"
      "v_exp_f32_e64 v92, v92 clamp\nv_and_b32_e32 v100, v232, v204\nv_exp_f32_e64 v93, v93 clamp\nv_and_b32_e32 v101, v233, v205\nv_exp_f32_e64 v94, v94 clamp\n"
      "v_xor_b32_e32 v204, v100, v204\nv_exp_f32_e64 v95, v95 clamp\nv_xor_b32_e32 v205, v101, v205\nv_cvt_pk_bf16_f32 v206, v92, v93\nv_cvt_pk_bf16_f32 v207, v94, v95\n"
      "v_and_b32_e32 v102, v234, v206\nv_and_b32_e32 v103, v235, v207\nv_xor_b32_e32 v206, v102, v206\nv_xor_b32_e32 v207, v103, v207\nv_exp_f32_e64 v64, v64 clamp\n"
      "v_exp_f32_e64 v65, v65 clamp\nv_mfma_f32_32x32x16_bf16 v[112:127], v[196:199], v[204:207], v[112:127]\nv_exp_f32_e64 v66, v66 clamp\nv_exp_f32_e64 v67, v67 clamp\n"
      "v_cvt_pk_bf16_f32 v200, v64, v65\nv_exp_f32_e64 v68, v68 clamp\nv_mfma_f32_32x32x16_bf16 v[48:63], v[152:155], v[184:187], v[128:143]\nv_exp_f32_e64 v69, v69 clamp\n"
      "v_cvt_pk_bf16_f32 v201, v66, v67\nv_exp_f32_e64 v70, v70 clamp\nv_exp_f32_e64 v71, v71 clamp\nv_mfma_f32_32x32x16_bf16 v[112:127], v[108:111], v[100:103], v[112:127]\n"
      "v_cvt_pk_bf16_f32 v202, v68, v69\nv_exp_f32_e64 v72, v72 clamp\nds_read_b64_tr_b16 v[196:197], v218 offset:256\nv_cvt_pk_bf16_f32 v203, v70, v71\nv_exp_f32_e64 v73, v73 clamp\n"
      "ds_read_b64_tr_b16 v[198:199], v218 offset:384\nv_exp_f32_e64 v74, v74 clamp\nds_read_b64_tr_b16 v[188:189], v219\nv_mfma_f32_32x32x16_bf16 v[112:127], v[192:195], v[200:203], v[112:127]\n"
      "v_exp_f32_e64 v75, v75 clamp\nds_read_b64_tr_b16 v[190:191], v219 offset:128\nv_cvt_pk_bf16_f32 v204, v72, v73\nv_exp_f32_e64 v76, v76 clamp\nv_exp_f32_e64 v77, v77 clamp\n"
      "v_cvt_pk_bf16_f32 v205, v74, v75\nv_exp_f32_e64 v78, v78 clamp\nv_exp_f32_e64 v79, v79 clamp\nv_cvt_pk_bf16_f32 v206, v76, v77\nv_exp_f32_e64 v48, v48 clamp\n"
      "v_exp_f32_e64 v49, v49 clamp\nv_cvt_pk_bf16_f32 v207, v78, v79\nv_mfma_f32_32x32x16_bf16 v[64:79], v[156:159], v[184:187], v[128:143]\nv_exp_f32_e64 v50, v50 clamp\n"
      "v_exp_f32_e64 v51, v51 clamp\nv_cvt_pk_bf16_f32 v200, v48, v49\nv_exp_f32_e64 v52, v52 clamp\nv_exp_f32_e64 v53, v53 clamp\ns_waitcnt lgkmcnt(0)\nv_mfma_f32_32x32x16_bf16 v[112:127], v[196:199], v[204:207], v[112:127]\n"
      "v_cvt_pk_bf16_f32 v201, v50, v51\nv_exp_f32_e64 v54, v54 clamp\nds_read_b64_tr_b16 v[196:197], v219 offset:256\nv_exp_f32_e64 v55, v55 clamp\nds_read_b64_tr_b16 v[198:199], v219 offset:384\n"
      "v_cvt_pk_bf16_f32 v202, v52, v53\nv_exp_f32_e64 v56, v56 clamp\nds_read_b64_tr_b16 v[192:193], v220\nv_cvt_pk_bf16_f32 v203, v54, v55\nv_exp_f32_e64 v57, v57 clamp\n"
      "ds_read_b64_tr_b16 v[194:195], v220 offset:128\nv_exp_f32_e64 v58, v58 clamp\nv_mfma_f32_32x32x16_bf16 v[112:127], v[188:191], v[200:203], v[112:127]\n"
      "v_exp_f32_e64 v59, v59 clamp\nv_cvt_pk_bf16_f32 v204, v56, v57\nv_exp_f32_e64 v60, v60 clamp\nv_exp_f32_e64 v61, v61 clamp\nv_cvt_pk_bf16_f32 v205, v58, v59\n"
      "v_exp_f32_e64 v62, v62 clamp\nv_exp_f32_e64 v63, v63 clamp\nv_cvt_pk_bf16_f32 v206, v60, v61\nv_exp_f32_e64 v64, v64 clamp\nv_exp_f32_e64 v65, v65 clamp\n"
      "v_cvt_pk_bf16_f32 v207, v62, v63\nv_mfma_f32_32x32x16_bf16 v[48:63], v[160:163], v[184:187], v[128:143]\nv_exp_f32_e64 v66, v66 clamp\nv_exp_f32_e64 v67, v67 clamp\n"
      "v_cvt_pk_bf16_f32 v200, v64, v65\nv_exp_f32_e64 v68, v68 clamp\nv_exp_f32_e64 v69, v69 clamp\ns_waitcnt lgkmcnt(0)\nv_mfma_f32_32x32x16_bf16 v[112:127], v[196:199], v[204:207], v[112:127]\n"
      "v_cvt_pk_bf16_f32 v201, v66, v67\nv_exp_f32_e64 v70, v70 clamp\nds_read_b64_tr_b16 v[196:197], v220 offset:256\nv_exp_f32_e64 v71, v71 clamp\nds_read_b64_tr_b16 v[198:199], v220 offset:384\n"
      "v_cvt_pk_bf16_f32 v202, v68, v69\nv_exp_f32_e64 v72, v72 clamp\nds_read_b64_tr_b16 v[188:189], v221\nv_cvt_pk_bf16_f32 v203, v70, v71\nv_exp_f32_e64 v73, v73 clamp\n"
      "ds_read_b64_tr_b16 v[190:191], v221 offset:128\nv_exp_f32_e64 v74, v74 clamp\nv_mfma_f32_32x32x16_bf16 v[112:127], v[192:195], v[200:203], v[112:127]\n"
      "v_exp_f32_e64 v75, v75 clamp\nv_cvt_pk_bf16_f32 v204, v72, v73\nv_exp_f32_e64 v76, v76 clamp\nv_exp_f32_e64 v77, v77 clamp\nv_cvt_pk_bf16_f32 v205, v74, v75\n"
      "v_exp_f32_e64 v78, v78 clamp\nv_exp_f32_e64 v79, v79 clamp\nv_cvt_pk_bf16_f32 v206, v76, v77\nv_exp_f32_e64 v48, v48 clamp\nv_exp_f32_e64 v49, v49 clamp\n"
      "v_cvt_pk_bf16_f32 v207, v78, v79\nv_mfma_f32_32x32x16_bf16 v[64:79], v[164:167], v[184:187], v[128:143]\nv_exp_f32_e64 v50, v50 clamp\nv_exp_f32_e64 v51, v51 clamp\n"
      "v_cvt_pk_bf16_f32 v200, v48, v49\nv_exp_f32_e64 v52, v52 clamp\nv_exp_f32_e64 v53, v53 clamp\ns_waitcnt lgkmcnt(0)\nv_mfma_f32_32x32x16_bf16 v[112:127], v[196:199], v[204:207], v[112:127]\n"
      "v_cvt_pk_bf16_f32 v201, v50, v51\nv_exp_f32_e64 v54, v54 clamp\nds_read_b64_tr_b16 v[196:197], v221 offset:256\nv_exp_f32_e64 v55, v55 clamp\nds_read_b64_tr_b16 v[198:199], v221 offset:384\n"
      "v_cvt_pk_bf16_f32 v202, v52, v53\nv_exp_f32_e64 v56, v56 clamp\nds_read_b64_tr_b16 v[192:193], v222\nv_cvt_pk_bf16_f32 v203, v54, v55\nv_exp_f32_e64 v57, v57 clamp\n"
      "ds_read_b64_tr_b16 v[194:195], v222 offset:128\nv_exp_f32_e64 v58, v58 clamp\nv_mfma_f32_32x32x16_bf16 v[112:127], v[188:191], v[200:203], v[112:127]\n"
      "v_exp_f32_e64 v59, v59 clamp\nv_cvt_pk_bf16_f32 v204, v56, v57\nv_exp_f32_e64 v60, v60 clamp\nv_exp_f32_e64 v61, v61 clamp\nv_cvt_pk_bf16_f32 v205, v58, v59\n"
      "v_exp_f32_e64 v62, v62 clamp\nv_exp_f32_e64 v63, v63 clamp\nv_cvt_pk_bf16_f32 v206, v60, v61\nv_exp_f32_e64 v64, v64 clamp\nv_exp_f32_e64 v65, v65 clamp\n"
      "v_cvt_pk_bf16_f32 v207, v62, v63\nv_mfma_f32_32x32x16_bf16 v[48:63], v[168:171], v[184:187], v[128:143]\nv_exp_f32_e64 v66, v66 clamp\nv_exp_f32_e64 v67, v67 clamp\n"
      "v_cvt_pk_bf16_f32 v200, v64, v65\nv_exp_f32_e64 v68, v68 clamp\nv_exp_f32_e64 v69, v69 clamp\ns_waitcnt lgkmcnt(0)\nv_mfma_f32_32x32x16_bf16 v[112:127], v[196:199], v[204:207], v[112:127]\n"
      "v_cvt_pk_bf16_f32 v201, v66, v67\nv_exp_f32_e64 v70, v70 clamp\nds_read_b64_tr_b16 v[196:197], v222 offset:256\nv_exp_f32_e64 v71, v71 clamp\nds_read_b64_tr_b16 v[198:199], v222 offset:384\n"
      "v_cvt_pk_bf16_f32 v202, v68, v69\nv_exp_f32_e64 v72, v72 clamp\nds_read_b64_tr_b16 v[188:189], v223\nv_cvt_pk_bf16_f32 v203, v70, v71\nv_exp_f32_e64 v73, v73 clamp\n"
      "ds_read_b64_tr_b16 v[190:191], v223 offset:128\nv_exp_f32_e64 v74, v74 clamp\nv_mfma_f32_32x32x16_bf16 v[112:127], v[192:195], v[200:203], v[112:127]\n"
      "v_exp_f32_e64 v75, v75 clamp\nv_cvt_pk_bf16_f32 v204, v72, v73\nv_exp_f32_e64 v76, v76 clamp\nv_exp_f32_e64 v77, v77 clamp\nv_cvt_pk_bf16_f32 v205, v74, v75\n"
      "v_exp_f32_e64 v78, v78 clamp\nv_exp_f32_e64 v79, v79 clamp\nv_cvt_pk_bf16_f32 v206, v76, v77\nv_exp_f32_e64 v48, v48 clamp\nv_exp_f32_e64 v49, v49 clamp\n"
      "v_cvt_pk_bf16_f32 v207, v78, v79\nv_mfma_f32_32x32x16_bf16 v[64:79], v[172:175], v[184:187], v[128:143]\nv_exp_f32_e64 v50, v50 clamp\nv_exp_f32_e64 v51, v51 clamp\n"
      "v_cvt_pk_bf16_f32 v200, v48, v49\nv_exp_f32_e64 v52, v52 clamp\nv_exp_f32_e64 v53, v53 clamp\ns_waitcnt lgkmcnt(0)\nv_mfma_f32_32x32x16_bf16 v[112:127], v[196:199], v[204:207], v[112:127]\n"
      "v_cvt_pk_bf16_f32 v201, v50, v51\nv_exp_f32_e64 v54, v54 clamp\nds_read_b64_tr_b16 v[196:197], v223 offset:256\nv_exp_f32_e64 v55, v55 clamp\nds_read_b64_tr_b16 v[198:199], v223 offset:384\n"
      "v_cvt_pk_bf16_f32 v202, v52, v53\nv_exp_f32_e64 v56, v56 clamp\nds_read_b64_tr_b16 v[192:193], v224\nv_cvt_pk_bf16_f32 v203, v54, v55\nv_exp_f32_e64 v57, v57 clamp\n"
      "ds_read_b64_tr_b16 v[194:195], v224 offset:128\nv_exp_f32_e64 v58, v58 clamp\nv_mfma_f32_32x32x16_bf16 v[112:127], v[188:191], v[200:203], v[112:127]\n"
      "v_exp_f32_e64 v59, v59 clamp\nv_cvt_pk_bf16_f32 v204, v56, v57\nv_exp_f32_e64 v60, v60 clamp\nv_exp_f32_e64 v61, v61 clamp\nv_cvt_pk_bf16_f32 v205, v58, v59\n"
      "v_exp_f32_e64 v62, v62 clamp\nv_exp_f32_e64 v63, v63 clamp\nv_cvt_pk_bf16_f32 v206, v60, v61\nv_exp_f32_e64 v64, v64 clamp\nv_exp_f32_e64 v65, v65 clamp\n"
      "v_cvt_pk_bf16_f32 v207, v62, v63\nv_exp_f32_e64 v66, v66 clamp\nv_exp_f32_e64 v67, v67 clamp\nv_cvt_pk_bf16_f32 v200, v64, v65\nv_exp_f32_e64 v68, v68 clamp\n"
      "v_exp_f32_e64 v69, v69 clamp\ns_waitcnt lgkmcnt(0)\nv_mfma_f32_32x32x16_bf16 v[112:127], v[196:199], v[204:207], v[112:127]\nv_cvt_pk_bf16_f32 v201, v66, v67\n"
      "v_exp_f32_e64 v70, v70 clamp\nds_read_b64_tr_b16 v[196:197], v224 offset:256\nv_exp_f32_e64 v71, v71 clamp\nds_read_b64_tr_b16 v[198:199], v224 offset:384\n"
      "v_cvt_pk_bf16_f32 v202, v68, v69\nv_exp_f32_e64 v72, v72 clamp\nv_cvt_pk_bf16_f32 v203, v70, v71\nv_exp_f32_e64 v73, v73 clamp\nv_exp_f32_e64 v74, v74 clamp\n"
      "v_mfma_f32_32x32x16_bf16 v[112:127], v[192:195], v[200:203], v[112:127]\nv_exp_f32_e64 v75, v75 clamp\nv_cvt_pk_bf16_f32 v204, v72, v73\nv_exp_f32_e64 v76, v76 clamp\n"
      "v_exp_f32_e64 v77, v77 clamp\nv_cvt_pk_bf16_f32 v205, v74, v75\nv_exp_f32_e64 v78, v78 clamp\nv_exp_f32_e64 v79, v79 clamp\nv_cvt_pk_bf16_f32 v206, v76, v77\n"
      "v_cvt_pk_bf16_f32 v207, v78, v79\ns_waitcnt lgkmcnt(0)\ns_nop 0\nv_mfma_f32_32x32x16_bf16 v[112:127], v[196:199], v[204:207], v[112:127]\ns_nop 11\nv_rcp_f32_e32 v239, v120\n"
      "s_nop 0\nv_mul_f32_e32 v112, v112, v239\nv_mul_f32_e32 v113, v113, v239\nv_mul_f32_e32 v114, v114, v239\nv_mul_f32_e32 v115, v115, v239\nv_mul_f32_e32 v116, v116, v239\n"
      "v_mul_f32_e32 v117, v117, v239\nv_mul_f32_e32 v118, v118, v239\nv_mul_f32_e32 v119, v119, v239\nv_cvt_pk_bf16_f32 v200, v112, v113\nv_cvt_pk_bf16_f32 v201, v114, v115\n"
      "v_cvt_pk_bf16_f32 v202, v116, v117\nv_cvt_pk_bf16_f32 v203, v118, v119\nv_min_u32_e32 v200, -2, v200\nv_min_u32_e32 v201, -2, v201\nv_min_u32_e32 v202, -2, v202\n"
      "v_min_u32_e32 v203, -2, v203\nbuffer_store_dwordx4 v[200:203], %[l16], %[rs], %[so] offen offset:1024 sc1\ns_nop 1\n"
      : "+{v[80:95]}"(bd), "+{v[96:111]}"(bf), "+{v[112:115]}"(st[0]), "+{v[116:119]}"(st[1]), "+{v[120:123]}"(st[2]), "+{v[124:127]}"(st[3]),
        "+{v[240:243]}"(st[4]), "+{v[244:247]}"(st[5]), "+{v[248:251]}"(st[6]), "+{v[252:255]}"(st[7])
      : "{v[180:183]}"(ag[0]), "{v[184:187]}"(ag[1]), "{v[208:223]}"(ad0), "{v[224:227]}"(ad1), "{v[228:231]}"(mk0), "{v[232:235]}"(mk1),
        [l16] "v"(l16), [rs] "s"(rs), [so] "s"(so), [sf] "s"(sf), [cn] "v"(cn), [one] "v"(one), [tv] "v"(tv), [pw] "v"(pw0), [np] "i"(APF)
      : "memory", "scc", "vcc",
        "v48", "v49", "v50", "v51", "v52", "v53", "v54", "v55", "v56", "v57", "v58", "v59", "v60", "v61", "v62", "v63", "v64", "v65", "v66", "v67", "v68", "v69", "v70",
        "v71", "v72", "v73", "v74", "v75", "v76", "v77", "v78", "v79", "v128", "v129", "v130", "v131", "v132", "v133", "v134", "v135", "v136", "v137", "v138", "v139",
        "v140", "v141", "v142", "v143", "v144", "v145", "v146", "v147", "v148", "v149", "v150", "v151", "v152", "v153", "v154", "v155", "v156", "v157", "v158", "v159",
        "v160", "v161", "v162", "v163", "v164", "v165", "v166", "v167", "v168", "v169", "v170", "v171", "v172", "v173", "v174", "v175", "v176", "v177", "v178", "v179",
        "v188", "v189", "v190", "v191", "v192", "v193", "v194", "v195", "v196", "v197", "v198", "v199", "v200", "v201", "v202", "v203", "v204", "v205", "v206", "v207",
        "v236", "v237", "v238", "v239");
    __builtin_amdgcn_s_setprio(OPRIO);
    HSTAMP(11); TSTAMP(5); SSTAMP(25);
    sl = abase + (kq >> 1) * 147456 + (kq & 1) * 16384;
    pad2 = (l16 << 3) + (WS_F1 + w * 64 * FS);
    asm volatile("s_cmp_eq_u32 %1, 0\n\ts_cbranch_scc0 1f\n\tbuffer_load_dword %0, %2, %3, 0 offen sc1\n1:" : "=&v"(epol) : "s"(b), "v"(pad2), "s"(rs) : "memory", "scc");
    if constexpr (ODLY) __builtin_amdgcn_s_sleep(ODLY);
    TSTAMP(6);
    lds_spin(LB + L_CNT + 8, 4);
#pragma unroll
    for (int k = 4; k < 8; k++) unstage(k, 0, k >> 1);
    cnt_add0(LB + L_CNT + 4, 1u);
    u32x4 wo_[8][2];
#pragma unroll
    for (int k = 0; k < 8; k++) if (!(k & 2)) { wo_[k][0] = w2frag(0, k, 0); wo_[k][1] = w2frag(0, k, 1); }
    TSTAMP(7);
    lds_spin(LB + L_CNT + 12, 12);
    if constexpr (OPRIO2 != OPRIO) __builtin_amdgcn_s_setprio(OPRIO2);
    TSTAMP(8);
    ops(IC<PA(5)>{});
    if constexpr (OPRIO3 != OPRIO2) __builtin_amdgcn_s_setprio(OPRIO3);
    TSTAMP(9);
    lds_spin(LB + L_CNT + 4, 4);
#pragma unroll
    for (int k = 0; k < 8; k++) if (k & 2) { wo_[k][0] = w2frag(0, k, 0); wo_[k][1] = w2frag(0, k, 1); }
    fin_addr(0);
    tie(xres);
#pragma unroll
    for (int i = 0; i < 4; i++) { c2[1][0][2 * i] = bflo(xres[i]); c2[1][0][2 * i + 1] = bfhi(xres[i]); }
    TSTAMP(10);
    wait8(oa_);
    tie(epol);
    if (__builtin_expect(b == 0, (OE >> 1) & 1)) check1(epol, T, pad2, rs);
    HSTAMP(12); TSTAMP(11);
    ocheck(IC<0>{});
    TSTAMP(12);
#pragma unroll
    for (int nt = 1; nt >= 0; nt--) {
#pragma unroll
      for (int k = 0; k < 8; k++) {
        sb();
        c2[0][nt] = mfma(wo_[k][nt], oa_[k], k ? c2[0][nt] : zero);
        sb();
        if (!nt && k >= 1 && k < 5) put(0, 8 - k);
      }
    }
    put(0, 3); put(0, 2); put(0, 1); put(0, 0);
    fin(IC<0>{});
    __builtin_amdgcn_endpgm();
  }
  {
    if constexpr (EPRIO) __builtin_amdgcn_s_setprio(EPRIO);
    ep3(IC<PG(0)>{});
    ops(IC<PG(2, 1)>{});
    sl = abase + kq * 16384;
    g.rw = __builtin_amdgcn_make_buffer_rsrc(sel(g.sw != 0, ws + WS_CP, (char*)g.wd), 0, 1792 * 1024, 0x00020000);
    g.we = kq * 196608; g.wl = kq * 262144;
    g.ro = __builtin_amdgcn_make_buffer_rsrc(sel(g.so != 0, ws + WS_CP + (1792 * 1024 >> CPD), (char*)g.wo), 0, 512 * 1024, 0x00020000);
    so = abase + kq * 8192;
    MSTAMP(10);
    wait_vm<vm_t(OKV, 5, PG(2, 1))>();
    MSTAMP(11);
    SBAR;
    __builtin_amdgcn_s_setprio(GPRIO);
    MSTAMP(12);
    HSTAMP(8); TSTAMP(2);
    ops(IC<PG(3)>{});
    constexpr int TS = 24;
    u32x4 wf[2][2], wt[32 - TS][2];
    wf[0][0] = w2frag(1, 0, 0); wf[0][1] = w2frag(1, 0, 1);
    auto gstep = [&](auto I_) {
      constexpr int i = decltype(I_)::v, P = SG_(i, 0);
      if constexpr (i == GPUB) { wait_vm<vm_h(OST, 5, P)>(); publish(); HSTAMP(9); TSTAMP(3); }
      else ops(IC<P>{});
      if constexpr (i == LCHK) { TSTAMP(5); wait_vm<vm_t(OPOL, 0, P)>(pol); TSTAMP(6); check1(pol, T, pad, rs); HSTAMP(10); TSTAMP(7); }
      constexpr int na = vm_t(OAG, 2 * i + 1, P), nw = i >= 5 + NS / 2 ? vm_t(OWG, 2 * i + (i == TS - 1 ? 5 : 3), P) : 63;
      if constexpr (imin(na, nw) < 63) wait_vm<imin(na, nw)>(ag[2 * i], ag[2 * i + 1]);
      if constexpr (i == 4) { TSTAMP(4); }
      if constexpr (i == 12) { HSTAMP(11); TSTAMP(8); }
      if constexpr (i == 16) { TSTAMP(9); }
      if constexpr (i == 20) { HSTAMP(12); TSTAMP(10); fin_addr(1); }
      if constexpr (i == 22) {
        tie(xres);
#pragma unroll
        for (int j = 0; j < 4; j++) { c2[0][0][2 * j] += bflo(xres[j]); c2[0][0][2 * j + 1] += bfhi(xres[j]); }
      }
      auto m1 = [&](auto J_) {
        constexpr int j = decltype(J_)::v, t = 3 - j;
        sb();
        c2[t >> 1][t & 1] = mfma(wf[i & 1][t & 1], ag[2 * i + (t >> 1)], i == 0 ? zero : c2[t >> 1][t & 1]);
        sb();
        if constexpr (i == TS - 1) wt[j >> 1][t & 1] = w2frag(1, TS + (j >> 1), t & 1);
        else if constexpr (j < 2) wf[(i + 1) & 1][t & 1] = w2frag(1, i + 1, t & 1);
        if constexpr (j >= 2 && i < NS / 2) unstage(2 * i + j - 2, 1, 6 + i);
        if constexpr (i * 4 + j == GD) __builtin_amdgcn_s_setprio(0);
        ops(IC<P + 1 + j>{});
        if constexpr (i * 4 + j == GSIG) cnt_add0(LB + L_CNT + 12, 1u);
        if constexpr (i * 4 + j == GU) __builtin_amdgcn_s_setprio(GPRIO);
      };
      m1(IC<0>{}); m1(IC<1>{}); m1(IC<2>{}); m1(IC<3>{});
    };
    gstep(IC<0>{}); gstep(IC<1>{}); gstep(IC<2>{}); gstep(IC<3>{}); gstep(IC<4>{}); gstep(IC<5>{}); gstep(IC<6>{});
    gstep(IC<7>{}); gstep(IC<8>{}); gstep(IC<9>{}); gstep(IC<10>{}); gstep(IC<11>{}); gstep(IC<12>{}); gstep(IC<13>{});
    gstep(IC<14>{}); gstep(IC<15>{}); gstep(IC<16>{}); gstep(IC<17>{}); gstep(IC<18>{}); gstep(IC<19>{}); gstep(IC<20>{});
    gstep(IC<21>{}); gstep(IC<22>{}); gstep(IC<23>{});
    TSTAMP(11);
    auto act = [&](int i, int t) -> u32x4& { return i < 28 ? ag[2 * i + t] : oa_[2 * (i - 28) + t]; };
    auto ochk4 = [&](auto T_) __attribute__((always_inline)) {
      constexpr int t = decltype(T_)::v, P = SG_(TS, 0);
      wait4<vm_t(OAG, 62 + t, P)>(oa_[t], oa_[2 + t], oa_[4 + t], oa_[6 + t]);
      if (__builtin_expect(unset4(oa_[t], oa_[2 + t], oa_[4 + t], oa_[6 + t]), (OE >> 2) & 1)) {
#pragma unroll 1
        for (int it = 0; it < SPIN; it++) {
          op(IC<op_find(OAG, 56 + t, P)>{}); op(IC<op_find(OAG, 58 + t, P)>{}); op(IC<op_find(OAG, 60 + t, P)>{}); op(IC<op_find(OAG, 62 + t, P)>{});
          wait4<0>(oa_[t], oa_[2 + t], oa_[4 + t], oa_[6 + t]);
          if (!unset4(oa_[t], oa_[2 + t], oa_[4 + t], oa_[6 + t])) break;
        }
      }
    };
    auto hstep = [&](auto I_) {
      constexpr int i = decltype(I_)::v, k = i - TS, P = SG_(TS, 0);
      if constexpr (i == 28) ochk4(IC<1>{});
      if constexpr (i < 28) wait_vm<imin(vm_t(OAG, 2 * i + 1, P), vm_t(OWG, 2 * i + 5, P))>(ag[2 * i + 1]);
      else if constexpr (i < 30) wait_vm<vm_t(OWG, 2 * i + 5, P)>();
#pragma unroll
      for (int nt = 1; nt >= 0; nt--) {
        sb();
        c2[1][nt] = mfma(wt[k][nt], act(i, 1), c2[1][nt]);
        sb();
        if constexpr (i + 2 < 32) wt[k + 2][nt] = w2frag(1, i + 2, nt);
      }
    };
    hstep(IC<24>{}); hstep(IC<25>{}); hstep(IC<26>{}); hstep(IC<27>{}); hstep(IC<28>{}); hstep(IC<29>{}); hstep(IC<30>{}); hstep(IC<31>{});
    TSTAMP(12);
#pragma unroll
    for (int nt = 1; nt >= 0; nt--) {
#pragma unroll
      for (int k = 0; k < 8; k++) {
        if (nt && k == 0) wait_vm<vm_t(OAG, 50, SG_(TS, 0))>(ag[48], ag[50]);
        if (nt && k == 2) wait_vm<vm_t(OAG, 54, SG_(TS, 0))>(ag[52], ag[54]);
        if (nt && k == 4) ochk4(IC<0>{});
        sb();
        c2[0][nt] = mfma(wt[k][nt], act(TS + k, 0), c2[0][nt]);
        sb();
        if (nt) put(1, 15 - k);
        else if (k >= 1 && k < 5) put(1, 8 - k);
      }
    }
    put(1, 3); put(1, 2);
    fin(IC<1>{});
  }
#endif
}

extern "C" void kernel_launch(void* const* d_in, const int* in_sizes, int n_in, void* d_out, int out_size, void* d_ws,
                              size_t ws_size, hipStream_t stream) {
  fused_block<<<256, 512, 0, stream>>>((const char*)d_in[0], (const char*)d_in[1], (const char*)d_in[2], (const char*)d_in[4],
                                       (const char*)d_in[5], (const char*)d_in[6], (char*)d_out, (char*)d_ws);
}
```
